```python
import torch

def _softmax(x, axis=-1):
    return torch.softmax(x, dim=axis)
def _gelu(x):
    return torch.nn.functional.gelu(x, approximate="tanh")
def _silu(x):
    return torch.nn.functional.silu(x)
def _sigmoid(x):
    return torch.sigmoid(x)
def _layernorm(x, axis=-1, eps=1e-5):
    m = x.mean(dim=axis, keepdim=True)
    v = x.var(dim=axis, keepdim=True, unbiased=False)
    return (x - m) / torch.sqrt(v + eps)
def _scatter_add(tgt, idx, src, axis=0):
    return tgt.clone().index_add_(axis, idx, src)
def _segment_sum(src, seg, n):
    out = torch.zeros((n,) + src.shape[1:], dtype=src.dtype)
    return out.index_add_(0, seg, src)
def _pad(x, pw):
    flat = tuple(v for pair in reversed(pw) for v in pair)
    return torch.nn.functional.pad(x, flat)

kernel_name = 'concat_heads__mha_block__linear_attn_block__prompt'

def setup_inputs():

    _w = (lambda a: (lambda t: t.to(torch.bfloat16) if t.is_floating_point() else t)(torch.as_tensor(a)))
    def _A_setup():
        B, S, D, H = 1, 2048, 1024, 32
        return {
            "x": _w((lambda *s: torch.randn(*s, dtype=torch.bfloat16) * 0.2)(B, S, D)),
            "wqkv": _w((lambda *s: torch.randn(*s, dtype=torch.bfloat16) * 0.2)(D, 3 * D)),
            "wo": _w((lambda *s: torch.randn(*s, dtype=torch.bfloat16) * 0.05)(D, D)),
        }
    _A_in = _A_setup()

    def _B_setup():
        B, S, D = 1, 2048, 1024
        return {
            "x": _w((lambda *s: torch.randn(*s, dtype=torch.bfloat16) * 0.05)(B, S, D)),
            "wqkv": _w((lambda *s: torch.randn(*s, dtype=torch.bfloat16) * 1.6)(D, 3 * D)),
            "wo": _w((lambda *s: torch.randn(*s, dtype=torch.bfloat16) * 0.1)(D, D)),
        }
    _B_in = _B_setup()

    return {
        "A_x": _A_in["x"],
        "A_wqkv": _A_in["wqkv"],
        "A_wo": _A_in["wo"],
        "B_x": _B_in["x"],
        "B_wqkv": _B_in["wqkv"],
        "B_wo": _B_in["wo"],
    }

def reference(A_x, A_wqkv, A_wo, B_x, B_wqkv, B_wo):
    def _A_ref(x, wqkv, wo):
        B, S, D = x.shape
        H = 32
        Dh = D // H
        qkv = (lambda x, *d: x.permute(*d))((x @ wqkv).reshape(B, S, 3, H, Dh), 2, 0, 3, 1, 4)
        q, k, v = qkv[0], qkv[1], qkv[2]
        scores = torch.einsum("bhqd,bhkd->bhqk", q, k) * (Dh ** -0.5)
        p = _softmax(scores, axis=-1)
        o = torch.einsum("bhqk,bhkd->bhqd", p, v)
        return (lambda x, *d: x.permute(*d))(o, 0, 2, 1, 3).reshape(B, S, D) @ wo

    def _B_ref(x, wqkv, wo):
        B, S, D = x.shape
        H = 32; Dh = D // H
        qkv = (lambda x, *d: x.permute(*d))((x @ wqkv).reshape(B, S, 3, H, Dh), 2, 0, 3, 1, 4)
        q, k, v = qkv[0], qkv[1], qkv[2]
        phi = lambda t: torch.where(t > 0, t + 1, torch.exp(t)) + 1e-6
        pq, pk = phi(q), phi(k)
        kv = torch.einsum("bhsd,bhse->bhde", pk, v)
        z = torch.einsum("bhsd->bhd", pk)
        num = torch.einsum("bhqd,bhde->bhqe", pq, kv)
        den = torch.einsum("bhqd,bhd->bhq", pq, z)[..., None] + 1e-6
        o = num / den
        return (lambda x, *d: x.permute(*d))(o, 0, 2, 1, 3).reshape(B, S, D) @ wo

    a = _A_ref(A_x, A_wqkv, A_wo)
    b = _B_ref(B_x, B_wqkv, B_wo)
    return torch.concatenate([a, b], axis=-1)
```

```cpp
#include <hip/hip_runtime.h>
#include <hip/hip_bf16.h>

typedef __attribute__((ext_vector_type(8))) short bf16x8;
typedef __attribute__((ext_vector_type(4))) short s16x4;
typedef __attribute__((ext_vector_type(16))) float f32x16;
typedef __attribute__((ext_vector_type(4))) float f32x4;
typedef __attribute__((ext_vector_type(2))) float f32x2;
typedef __attribute__((ext_vector_type(4))) unsigned u32x4;
typedef __attribute__((address_space(3))) char lds_char;
#define LDSP(T, p) ((__attribute__((address_space(3))) T*)(p))

#define DI __device__ __forceinline__
#define MFMA32(a, b, c) __builtin_amdgcn_mfma_f32_32x32x16_bf16((a), (b), (c), 0, 0, 0)
#define MFMA16(a, b, c) __builtin_amdgcn_mfma_f32_16x16x32_bf16((a), (b), (c), 0, 0, 0)
#define SBAR() __builtin_amdgcn_sched_barrier(0)
#define PIN(x) asm volatile("" : "+v"(x))
#define WAIT_BAR(N) asm volatile("s_waitcnt vmcnt(" #N ") lgkmcnt(0)\n\ts_barrier" ::: "memory")

constexpr int SEQ = 2048, DM = 1024, NH = 32;
constexpr float LOG2E = 1.4426950408889634f;
constexpr float QSCALE = 0.17677669529663687f * LOG2E;

constexpr size_t WS_Q = 0;
constexpr size_t WS_KV = (size_t)4 << 20;
constexpr size_t WS_PQ = (size_t)12 << 20;
constexpr size_t WS_O = (size_t)16 << 20;
constexpr size_t WS_KVP = (size_t)24 << 20;
constexpr size_t WS_ZP = (size_t)25 << 20;

typedef __attribute__((ext_vector_type(2))) __bf16 bf16x2_t;
DI unsigned cvtpk(float lo, float hi) {
    return __builtin_bit_cast(unsigned, __builtin_convertvector((f32x2{lo, hi}), bf16x2_t));
}
DI bf16x8 pack8(const f32x16& x, int s) {
    u32x4 p;
    p[0] = cvtpk(x[8 * s + 0], x[8 * s + 1]);
    p[1] = cvtpk(x[8 * s + 2], x[8 * s + 3]);
    p[2] = cvtpk(x[8 * s + 4], x[8 * s + 5]);
    p[3] = cvtpk(x[8 * s + 6], x[8 * s + 7]);
    return __builtin_bit_cast(bf16x8, p);
}
DI void dma16(__amdgpu_buffer_rsrc_t rsrc, unsigned voff, unsigned soff, unsigned lds_dst) {
    unsigned keep;
    asm volatile("s_mov_b32 %0, m0\n\ts_mov_b32 m0, %4\n\ts_nop 0\n\tbuffer_load_dwordx4 %1, %2, %3 offen lds\n\ts_mov_b32 m0, %0"
                 : "=&s"(keep) : "v"(voff), "s"(rsrc), "s"(soff), "s"(lds_dst) : "memory");
}
DI void dma16_lo(__amdgpu_buffer_rsrc_t rsrc, unsigned voff, unsigned soff, unsigned lds_dst) {
    unsigned keep;
    asm volatile("s_mov_b32 %0, m0\n\ts_mov_b32 m0, %4\n\ts_mov_b32 exec_hi, 0\n\tbuffer_load_dwordx4 %1, %2, %3 offen lds\n\ts_mov_b32 exec_hi, -1\n\ts_mov_b32 m0, %0"
                 : "=&s"(keep) : "v"(voff), "s"(rsrc), "s"(soff), "s"(lds_dst) : "memory");
}
DI __amdgpu_buffer_rsrc_t mk_rsrc(const void* p, unsigned bytes) {
    return __builtin_amdgcn_make_buffer_rsrc((void*)p, (short)0, (int)bytes, 0x00020000);
}
DI bf16x8 lds_rd128(const lds_char* p) { return *LDSP(const bf16x8, p); }
DI s16x4 lds_tr(const lds_char* p) {
    typedef short v4i16_t __attribute__((ext_vector_type(4)));
    return __builtin_bit_cast(s16x4, __builtin_amdgcn_ds_read_tr16_b64_v4i16(LDSP(v4i16_t, p)));
}
DI bf16x8 cat8(s16x4 lo, s16x4 hi) { return __builtin_shufflevector(lo, hi, 0, 1, 2, 3, 4, 5, 6, 7); }
DI unsigned rfl(unsigned x) { return __builtin_amdgcn_readfirstlane(x); }

constexpr int K1_ASLOT = 32768, K1_BSLOT = 12288, K1_BOFF = 3 * K1_ASLOT, K1_LDS = K1_BOFF + 5 * K1_BSLOT;

struct K1Frag { bf16x8 a[2]; bf16x8 b[3]; };

__global__ __launch_bounds__(512, 2) void qkv_kernel(const unsigned short* __restrict__ xA, const unsigned short* __restrict__ wA,
                                                     const unsigned short* __restrict__ xB, const unsigned short* __restrict__ wB,
                                                     char* __restrict__ ws) {
    extern __shared__ __attribute__((aligned(16))) char smem[];
    const lds_char* lds = (const lds_char*)smem;
    const unsigned lds0 = (unsigned)(size_t)lds;
    const int tid = threadIdx.x, lane = tid & 63;
    const int wid = rfl(tid >> 6), wm = wid & 3, hh = wid >> 2;
    const int r32 = lane & 31, hi = lane >> 5;
    const int xg = blockIdx.x & 7, j = blockIdx.x >> 3;
    const int br = xg >> 2;
    const int mb = (xg & 1) * 4 + (j & 3);
    const int hp = ((xg >> 1) & 1) * 8 + (j >> 2);
    const unsigned short* X = br ? xB : xA;
    const unsigned short* W = br ? wB : wA;
    const __amdgpu_buffer_rsrc_t rX = mk_rsrc(X + (size_t)mb * 256 * DM, 256 * DM * 2);
    const __amdgpu_buffer_rsrc_t rW = mk_rsrc(W, DM * 3 * DM * 2);

    unsigned voA[4];
#pragma unroll
    for (int i = 0; i < 4; ++i) {
        const int R = wid * 32 + i * 8 + (lane >> 3);
        const int c = (lane & 7) ^ ((R >> 1) & 7);
        voA[i] = R * (DM * 2) + c * 16;
    }
    unsigned voB[2];
    {
        const int q = (lane >> 2) & 3, p = lane & 3;
#pragma unroll
        for (int i = 0; i < 2; ++i) {
            const int cb = i * 4 + (lane >> 4);
            const int cbc = cb > 5 ? 5 : cb;
            voB[i] = (wid * 4 + q) * (3 * DM * 2) + ((cbc >> 1) * DM + hp * 64 + (cbc & 1) * 32 + p * 8) * 2;
        }
    }
    const unsigned dA = lds0 + wid * 4096;
    const unsigned dB = lds0 + K1_BOFF + wid * 1536;
#define K1_DMA_A(t, slot) do { _Pragma("unroll") for (int i_ = 0; i_ < 4; ++i_) dma16(rX, voA[i_], (unsigned)(t) * 128u, dA + (slot) + i_ * 1024); } while (0)
#define K1_DMA_B(u, slot) do { dma16(rW, voB[0], (unsigned)(u) * (32u * 3 * DM * 2), dB + (slot)); dma16_lo(rW, voB[1], (unsigned)(u) * (32u * 3 * DM * 2), dB + (slot) + 1024); } while (0)

    K1_DMA_B(0, 0); K1_DMA_A(0, 0);
    K1_DMA_B(1, K1_BSLOT);
    K1_DMA_B(2, 2 * K1_BSLOT); K1_DMA_A(1, K1_ASLOT);
    K1_DMA_B(3, 3 * K1_BSLOT);
    K1_DMA_B(4, 4 * K1_BSLOT); K1_DMA_A(2, 2 * K1_ASLOT);

    unsigned rdA[4];
#pragma unroll
    for (int s = 0; s < 4; ++s) rdA[s] = (wm * 64 + r32) * 128 + (((2 * s + hi) ^ ((r32 >> 1) & 7)) * 16);
    const unsigned rdB = K1_BOFF + hi * (2 * 1536) + ((lane & 15) >> 2) * 64 + ((lane >> 4) & 1) * 32 + (lane & 3) * 8 + hh * 256;

    f32x16 acc[2][3];
    {
        const float init = br ? 1e-6f : 0.f;
#pragma unroll
        for (int i = 0; i < 2; ++i)
#pragma unroll
            for (int r = 0; r < 16; ++r) { acc[i][0][r] = init; acc[i][1][r] = init; acc[i][2][r] = 0.f; }
    }
    K1Frag F0, F1;
#define RDA(F, i, ks, sa) F.a[i] = lds_rd128(lds + (sa) + rdA[ks] + (i) * 4096)
#define RDB(F, c, sp, sb) F.b[c] = cat8(lds_tr(lds + (sb) + rdB + (sp) * 6144 + (c) * 512), lds_tr(lds + (sb) + rdB + (sp) * 6144 + 1536 + (c) * 512))
#define MM(F, i, c) do { \
        if ((c) == 0) acc[i][c] = MFMA32(F.b[c], F.a[i], acc[i][c]); \
        else if ((c) == 2) acc[i][c] = MFMA32(F.a[i], F.b[c], acc[i][c]); \
        else { if (BR) acc[i][c] = MFMA32(F.a[i], F.b[c], acc[i][c]); else acc[i][c] = MFMA32(F.b[c], F.a[i], acc[i][c]); } } while (0)
#define STEP(FC, FN, ks, sa, sp, sb, MID) do { \
        MM(FC, 0, 0); SBAR(); RDA(FN, 0, ks, sa); RDB(FN, 0, sp, sb); SBAR(); \
        MM(FC, 1, 0); SBAR(); RDA(FN, 1, ks, sa); RDB(FN, 1, sp, sb); SBAR(); \
        MM(FC, 0, 1); SBAR(); RDB(FN, 2, sp, sb); SBAR(); \
        MM(FC, 1, 1); SBAR(); MID; SBAR(); \
        MM(FC, 0, 2); MM(FC, 1, 2); SBAR(); } while (0)
#define STEP_LAST(FC) do { MM(FC, 0, 0); MM(FC, 1, 0); MM(FC, 0, 1); MM(FC, 1, 1); MM(FC, 0, 2); MM(FC, 1, 2); } while (0)

    auto mainloop = [&](auto brc) {
        constexpr bool BR = decltype(brc)::value;
        WAIT_BAR(16);
        RDA(F0, 0, 0, 0); RDA(F0, 1, 0, 0); RDB(F0, 0, 0, 0); RDB(F0, 1, 0, 0); RDB(F0, 2, 0, 0);
        unsigned sa = 0, sb = 0;
#define NEXT_A(s) ((s) == 2 * K1_ASLOT ? 0u : (s) + K1_ASLOT)
#define NEXT_B(s) ((s) == 4 * K1_BSLOT ? 0u : (s) + K1_BSLOT)
#define TILE(t, W0, W1, ISS0, ISS1, LAST) do { \
            const unsigned sb1 = NEXT_B(sb), sb2 = NEXT_B(sb1), sa1 = NEXT_A(sa); \
            STEP(F0, F1, 1, sa, 1, sb, (void)0); \
            WAIT_BAR(W0); SBAR(); \
            STEP(F1, F0, 2, sa, 0, sb1, if (ISS0) K1_DMA_B(2 * (t) + 5, sb)); \
            STEP(F0, F1, 3, sa, 1, sb1, (void)0); \
            if (!(LAST)) { \
                WAIT_BAR(W1); SBAR(); \
                STEP(F1, F0, 0, sa1, 0, sb2, if (ISS1) { K1_DMA_B(2 * (t) + 6, sb1); K1_DMA_A((t) + 3, sa); }); \
            } else { STEP_LAST(F1); } \
            sa = sa1; sb = sb2; } while (0)
        for (int t = 0; t < 13; ++t) TILE(t, 14, 10, true, true, false);
        TILE(13, 14, 10, true, false, false);
        TILE(14, 8, 2, false, false, false);
        TILE(15, 0, 0, false, false, true);
    };
    if (br) mainloop(std::integral_constant<bool, true>{}); else mainloop(std::integral_constant<bool, false>{});

    const int head = hp * 2 + hh;
    const int tile0 = mb * 8 + wm * 2;
    if (!br) {
        u32x4* Qf = (u32x4*)(ws + WS_Q);
        u32x4* KVf = (u32x4*)(ws + WS_KV);
#pragma unroll
        for (int i = 0; i < 2; ++i) {
            const int tl = head * 64 + tile0 + i;
            f32x16 q = acc[i][0] * QSCALE;
#pragma unroll
            for (int s = 0; s < 2; ++s) {
                Qf[(tl * 2 + s) * 64 + lane] = __builtin_bit_cast(u32x4, pack8(q, s));
                KVf[(tl * 4 + s) * 64 + lane] = __builtin_bit_cast(u32x4, pack8(acc[i][1], s));
                KVf[(tl * 4 + 2 + s) * 64 + lane] = __builtin_bit_cast(u32x4, pack8(acc[i][2], s));
            }
        }
    } else {
        u32x4* PQf = (u32x4*)(ws + WS_PQ);
        auto phi = [](f32x16& v) {
#pragma unroll
            for (int r = 0; r < 16; ++r) {
                const float u = __builtin_fmaf(v[r], LOG2E, -1e-6f * LOG2E);
                const float e = __builtin_fminf(__builtin_amdgcn_exp2f(u), 1.0f);
                v[r] = __builtin_fmaxf(v[r], 1e-6f) + e;
            }
        };
        f32x16 kv = {}, zz = {};
        bf16x8 ones;
#pragma unroll
        for (int e = 0; e < 8; ++e) ones[e] = (short)0x3F80;
#pragma unroll
        for (int i = 0; i < 2; ++i) {
            const int tl = head * 64 + tile0 + i;
            phi(acc[i][0]);
            phi(acc[i][1]);
#pragma unroll
            for (int s = 0; s < 2; ++s) {
                PQf[(tl * 2 + s) * 64 + lane] = __builtin_bit_cast(u32x4, pack8(acc[i][0], s));
                const bf16x8 pk = pack8(acc[i][1], s), vv = pack8(acc[i][2], s);
                kv = MFMA32(pk, vv, kv);
                zz = MFMA32(pk, ones, zz);
            }
        }
        asm volatile("s_waitcnt lgkmcnt(0)\n\ts_barrier" ::: "memory");
        lds_char* red = (lds_char*)smem;
#pragma unroll
        for (int p = 0; p < 8; ++p) *LDSP(f32x2, red + wid * 4096 + p * 512 + lane * 8) = f32x2{kv[2 * p], kv[2 * p + 1]};
        if (r32 == 0) {
#pragma unroll
            for (int g = 0; g < 4; ++g) *LDSP(f32x4, red + 32768 + wid * 128 + hi * 64 + g * 16) = f32x4{zz[4 * g], zz[4 * g + 1], zz[4 * g + 2], zz[4 * g + 3]};
        }
        asm volatile("s_waitcnt lgkmcnt(0)\n\ts_barrier" ::: "memory");
        {
            const int h2 = tid >> 8, idx = tid & 255;
            f32x4 s4 = *LDSP(const f32x4, red + (h2 * 4 + 0) * 4096 + idx * 16);
#pragma unroll
            for (int m = 1; m < 4; ++m) s4 += *LDSP(const f32x4, red + (h2 * 4 + m) * 4096 + idx * 16);
            ((f32x4*)(ws + WS_KVP))[((hp * 2 + h2) * 8 + mb) * 256 + idx] = s4;
            if (tid < 64) {
                const int h3 = tid >> 5, i3 = tid & 31;
                float z = 0.f;
#pragma unroll
                for (int m = 0; m < 4; ++m) z += *LDSP(const float, red + 32768 + (h3 * 4 + m) * 128 + i3 * 4);
                ((float*)(ws + WS_ZP))[((hp * 2 + h3) * 8 + mb) * 32 + i3] = z;
            }
        }
    }
#undef RDA
#undef RDB
#undef MM
#undef STEP
#undef STEP_LAST
#undef TILE
}

constexpr int K2_SLOT = 16384, K2_NSLOT = 8, K2_XOFF = K2_SLOT * K2_NSLOT, K2_LDS = K2_XOFF + 4096;

DI float xhalf_max(float m) {
    auto rr = __builtin_amdgcn_permlane32_swap(__float_as_uint(m), __float_as_uint(m), false, false);
    return __builtin_fmaxf(__uint_as_float(rr[0]), __uint_as_float(rr[1]));
}
DI float xhalf_sum(float m) {
    auto rr = __builtin_amdgcn_permlane32_swap(__float_as_uint(m), __float_as_uint(m), false, false);
    return __uint_as_float(rr[0]) + __uint_as_float(rr[1]);
}
DI float max16(const f32x16& s) {
    float a = __builtin_fmaxf(s[0], s[1]);
#pragma unroll
    for (int r = 2; r < 16; ++r) a = __builtin_fmaxf(a, s[r]);
    return a;
}

__global__ __launch_bounds__(512, 2) void attn_kernel(char* __restrict__ ws) {
    extern __shared__ __attribute__((aligned(16))) char smem[];
    const lds_char* lds = (const lds_char*)smem;
    const unsigned lds0 = (unsigned)(size_t)lds;
    const int tid = threadIdx.x, lane = tid & 63;
    const int wid = rfl(tid >> 6);
    const int hi = lane >> 5;
    const int xg = blockIdx.x & 7, j0 = blockIdx.x >> 3;
    const int head = xg * 4 + (j0 >> 3), qb = j0 & 7;
    const int qt = qb * 8 + wid;

    const __amdgpu_buffer_rsrc_t rKV = mk_rsrc(ws + WS_KV + (size_t)head * 64 * 4096, 64 * 4096);
    const unsigned voKV = wid * 2048 + lane * 16;
    const unsigned dKV = lds0 + wid * 2048;
#define K2_DMA(st) do { dma16(rKV, voKV, (unsigned)(st) * K2_SLOT, dKV + ((st) & 7) * K2_SLOT); dma16(rKV, voKV + 1024, (unsigned)(st) * K2_SLOT, dKV + ((st) & 7) * K2_SLOT + 1024); } while (0)

    const u32x4* Qf = (const u32x4*)(ws + WS_Q) + (size_t)(head * 64 + qt) * 128 + lane;
    const bf16x8 q0 = __builtin_bit_cast(bf16x8, Qf[0]), q1 = __builtin_bit_cast(bf16x8, Qf[64]);
    f32x2 kvp[8], zp[8];
    {
        const f32x2* KVP = (const f32x2*)(ws + WS_KVP) + (size_t)head * 8 * 512 + wid * 64 + lane;
        const f32x2* ZP = (const f32x2*)(ws + WS_ZP) + head * 8 * 16 + hi * 8 + wid;
#pragma unroll
        for (int m = 0; m < 8; ++m) { kvp[m] = KVP[m * 512]; zp[m] = ZP[m * 16]; }
    }
    const u32x4* PQf = (const u32x4*)(ws + WS_PQ) + (size_t)(head * 64 + qt) * 128 + lane;
    const bf16x8 pq0 = __builtin_bit_cast(bf16x8, PQf[0]), pq1 = __builtin_bit_cast(bf16x8, PQf[64]);
    asm volatile("" ::: "memory");
#pragma unroll
    for (int st = 0; st < 8; ++st) K2_DMA(st);
    {
        f32x2 a = kvp[0], z = zp[0];
#pragma unroll
        for (int m = 1; m < 8; ++m) { a += kvp[m]; z += zp[m]; }
        lds_char* xs = (lds_char*)smem + K2_XOFF + (wid >> 2) * 1024 + lane * 16 + (wid & 3) * 4;
        *LDSP(unsigned, xs) = cvtpk(a[0], a[1]);
        *LDSP(unsigned, xs + 2048) = cvtpk(z[0], z[1]);
    }
    WAIT_BAR(14);
    {
        const lds_char* xs = lds + K2_XOFF + lane * 16;
        const bf16x8 kv0 = lds_rd128(xs), kv1 = lds_rd128(xs + 1024), z0 = lds_rd128(xs + 2048), z1 = lds_rd128(xs + 3072);
        f32x16 num = {}, den = {};
        num = MFMA32(kv0, pq0, num); den = MFMA32(z0, pq0, den);
        num = MFMA32(kv1, pq1, num); den = MFMA32(z1, pq1, den);
        const float rd = 1.0f / (den[0] + 1e-6f);
        num *= rd;
        u32x4* Of = (u32x4*)(ws + WS_O) + (size_t)((64 + qt) * 32 + head) * 128 + lane;
        Of[0] = __builtin_bit_cast(u32x4, pack8(num, 0));
        Of[64] = __builtin_bit_cast(u32x4, pack8(num, 1));
    }

    bf16x8 sel;
    {
        const short v = (((lane >> 4) & 1) == (((lane & 15) >> 2) & 1)) ? (short)0x3F80 : (short)0;
#pragma unroll
        for (int e = 0; e < 8; ++e) sel[e] = v;
    }
    const lds_char* fb = lds + lane * 16;
    bf16x8 k0, k1, v0, v1;
    f32x16 sA, sB, negm, o = {};
    f32x4 lsum = {};
    u32x4 p0, p1 = {};
    k0 = lds_rd128(fb); k1 = lds_rd128(fb + 1024);
    {
        f32x16 z16 = {};
        sA = MFMA32(k0, q0, z16); sA = MFMA32(k1, q1, sA);
        const float m = xhalf_max(max16(sA));
#pragma unroll
        for (int r = 0; r < 16; ++r) { negm[r] = -m; sA[r] -= m; }
    }
    PIN(negm);
    k0 = lds_rd128(fb + 4096); k1 = lds_rd128(fb + 4096 + 1024);
    v0 = lds_rd128(fb + 2048); v1 = lds_rd128(fb + 3072);

#define EX(X, i) X[i] = __builtin_amdgcn_exp2f(X[i])
#define BF(x) __builtin_bit_cast(bf16x8, x)
#define ATT(SC, SN, KP, VP, DO_QK) do { \
        if (DO_QK) SN = MFMA32(k0, q0, negm); \
        EX(SC, 0); EX(SC, 1); p0[0] = cvtpk(SC[0], SC[1]); EX(SC, 2); PIN(SC); PIN(p0); SBAR(); \
        if (DO_QK) { SN = MFMA32(k1, q1, SN); } \
        EX(SC, 3); p0[1] = cvtpk(SC[2], SC[3]); EX(SC, 4); EX(SC, 5); PIN(SC); PIN(p0); SBAR(); \
        lsum = MFMA16(sel, BF(p1), lsum); \
        if (DO_QK) { k0 = lds_rd128(KP); k1 = lds_rd128((KP) + 1024); } \
        p0[2] = cvtpk(SC[4], SC[5]); EX(SC, 6); EX(SC, 7); p0[3] = cvtpk(SC[6], SC[7]); EX(SC, 8); PIN(SC); PIN(p0); SBAR(); \
        o = MFMA32(v0, BF(p0), o); \
        EX(SC, 9); p1[0] = cvtpk(SC[8], SC[9]); EX(SC, 10); EX(SC, 11); PIN(SC); PIN(p1); SBAR(); \
        lsum = MFMA16(sel, BF(p0), lsum); \
        if (DO_QK) v0 = lds_rd128(VP); \
        p1[1] = cvtpk(SC[10], SC[11]); EX(SC, 12); EX(SC, 13); p1[2] = cvtpk(SC[12], SC[13]); EX(SC, 14); EX(SC, 15); p1[3] = cvtpk(SC[14], SC[15]); PIN(p1); SBAR(); \
        o = MFMA32(v1, BF(p1), o); \
        if (DO_QK) v1 = lds_rd128((VP) + 1024); \
        SBAR(); } while (0)

    ATT(sA, sB, fb + 2 * 4096, fb + 1 * 4096 + 2048, true);
    ATT(sB, sA, fb + 3 * 4096, fb + 2 * 4096 + 2048, true);
    for (int k = 1; k < 16; ++k) {
        if (k < 10) WAIT_BAR(6); else WAIT_BAR(0);
        if (k >= 2 && k < 10) K2_DMA(k + 6);
        SBAR();
        const lds_char* cur = fb + (k & 7) * K2_SLOT;
        const lds_char* prv = fb + ((k - 1) & 7) * K2_SLOT;
        ATT(sA, sB, cur, prv + 3 * 4096 + 2048, true);
        ATT(sB, sA, cur + 4096, cur + 2048, true);
        ATT(sA, sB, cur + 2 * 4096, cur + 4096 + 2048, true);
        ATT(sB, sA, cur + 3 * 4096, cur + 2 * 4096 + 2048, true);
    }
    {
        const lds_char* cur = fb + 7 * K2_SLOT;
        ATT(sA, sB, cur, cur + 3 * 4096 + 2048, true);
        ATT(sB, sA, cur, cur, false);
        lsum = MFMA16(sel, BF(p1), lsum);
    }
    float l = lsum[0];
    {
        float t = 0.f;
#pragma unroll
        for (int r = 0; r < 16; ++r) t = __builtin_fmaxf(t, __builtin_fabsf(o[r]));
        const bool nan_o = [&] { bool b = false;
#pragma unroll
            for (int r = 0; r < 16; ++r) b |= (o[r] != o[r]);
            return b; }();
        const bool bad = !(l < 1.8446744e19f) || !(t < 3.0e38f) || nan_o;
        if (__builtin_expect(__any(bad), 0)) {
            const u32x4* KV = (const u32x4*)(ws + WS_KV) + (size_t)head * 64 * 256 + lane;
            float m = -INFINITY, ls = 0.f;
#pragma unroll
            for (int r = 0; r < 16; ++r) o[r] = 0.f;
            for (int jt = 0; jt < 64; ++jt) {
                const bf16x8 a0 = BF(KV[jt * 256]), a1 = BF(KV[jt * 256 + 64]), b0 = BF(KV[jt * 256 + 128]), b1 = BF(KV[jt * 256 + 192]);
                f32x16 s = {};
                s = MFMA32(a0, q0, s); s = MFMA32(a1, q1, s);
                const float mn = __builtin_fmaxf(m, xhalf_max(max16(s)));
                const float al = __builtin_amdgcn_exp2f(m - mn);
                m = mn; ls *= al;
#pragma unroll
                for (int r = 0; r < 16; ++r) { o[r] *= al; s[r] = __builtin_amdgcn_exp2f(s[r] - mn); ls += s[r]; }
                o = MFMA32(b0, pack8(s, 0), o); o = MFMA32(b1, pack8(s, 1), o);
            }
            l = xhalf_sum(ls);
        }
    }
    {
        const float rl = 1.0f / l;
        o *= rl;
        u32x4* Of = (u32x4*)(ws + WS_O) + (size_t)(qt * 32 + head) * 128 + lane;
        Of[0] = __builtin_bit_cast(u32x4, pack8(o, 0));
        Of[64] = __builtin_bit_cast(u32x4, pack8(o, 1));
    }
#undef EX
#undef ATT
#undef K2_DMA
}

constexpr int K3_SLOT = 32768, K3_NSLOT = 4, K3_RP = 528, K3_LDS = 2 * 128 * K3_RP;

__global__ __launch_bounds__(512, 2) void oproj_kernel(const unsigned short* __restrict__ woA, const unsigned short* __restrict__ woB,
                                                       const char* __restrict__ ws, unsigned short* __restrict__ out) {
    extern __shared__ __attribute__((aligned(16))) char smem[];
    const lds_char* lds = (const lds_char*)smem;
    const unsigned lds0 = (unsigned)(size_t)lds;
    const int tid = threadIdx.x, lane = tid & 63;
    const int wid = rfl(tid >> 6), rt = wid & 3, kg = wid >> 2;
    const int r32 = lane & 31, hi = lane >> 5;
    const int xg = blockIdx.x & 7, j = blockIdx.x >> 3;
    const int br = xg >> 2, rb = (xg & 3) * 4 + (j >> 3), cb = j & 7;
    const unsigned short* Wo = br ? woB : woA;
    const __amdgpu_buffer_rsrc_t rW = mk_rsrc(Wo, DM * DM * 2);
    const __amdgpu_buffer_rsrc_t rO = mk_rsrc(ws + WS_O + (size_t)(br * 64 + rb * 4) * 65536, 4 * 65536);
    const unsigned voW = (kg * 512 + rt * 8 + ((lane >> 2) & 3)) * (DM * 2) + (cb * 128 + (lane >> 4) * 32 + (lane & 3) * 8) * 2;
    const unsigned dW = lds0 + kg * 8192 + rt * 2048;
    const unsigned voO = rt * 65536 + kg * 32768 + lane * 16;
    const unsigned dO = lds0 + 16384 + wid * 2048;
#define K3_DMA(i) do { const unsigned sl_ = ((i) & 3) * K3_SLOT; \
        dma16(rW, voW, (unsigned)(i) * (32u * DM * 2), dW + sl_); dma16(rW, voW + 4 * DM * 2, (unsigned)(i) * (32u * DM * 2), dW + sl_ + 1024); \
        dma16(rO, voO, (unsigned)(i) * 2048u, dO + sl_); dma16(rO, voO + 1024, (unsigned)(i) * 2048u, dO + sl_ + 1024); } while (0)
    K3_DMA(0); K3_DMA(1); K3_DMA(2);
    const unsigned rdW = kg * 8192 + hi * 1024 + ((lane & 15) >> 2) * 64 + ((lane >> 4) & 1) * 32 + (lane & 3) * 8;
    const unsigned rdO = 16384 + wid * 2048 + lane * 16;
    f32x16 acc[4] = {};
    for (int i = 0; i < 16; ++i) {
        if (i < 14) WAIT_BAR(8); else if (i == 14) WAIT_BAR(4); else WAIT_BAR(0);
        if (i + 3 < 16) K3_DMA(i + 3);
        const lds_char* sl = lds + (i & 3) * K3_SLOT;
#pragma unroll
        for (int s = 0; s < 2; ++s) {
            const bf16x8 of = lds_rd128(sl + rdO + s * 1024);
#pragma unroll
            for (int c = 0; c < 4; ++c) {
                const bf16x8 wf = cat8(lds_tr(sl + rdW + s * 4096 + c * 256), lds_tr(sl + rdW + s * 4096 + 2048 + c * 256));
                acc[c] = MFMA32(wf, of, acc[c]);
            }
        }
    }
    asm volatile("s_waitcnt lgkmcnt(0)\n\ts_barrier" ::: "memory");
    {
        lds_char* rw = (lds_char*)smem + (kg * 128 + rt * 32 + r32) * K3_RP + hi * 16;
#pragma unroll
        for (int c = 0; c < 4; ++c)
#pragma unroll
            for (int g = 0; g < 4; ++g)
                *LDSP(f32x4, rw + c * 128 + g * 32) = f32x4{acc[c][4 * g], acc[c][4 * g + 1], acc[c][4 * g + 2], acc[c][4 * g + 3]};
    }
    asm volatile("s_waitcnt lgkmcnt(0)\n\ts_barrier" ::: "memory");
#pragma unroll
    for (int it = 0; it < 4; ++it) {
        const int row = it * 32 + (tid >> 4), ch = tid & 15;
        const lds_char* p = lds + row * K3_RP + ch * 32;
        const f32x4 a0 = *LDSP(const f32x4, p), a1 = *LDSP(const f32x4, p + 16);
        const f32x4 b0 = *LDSP(const f32x4, p + 128 * K3_RP), b1 = *LDSP(const f32x4, p + 128 * K3_RP + 16);
        const f32x4 s0 = a0 + b0, s1 = a1 + b1;
        u32x4 w;
        w[0] = cvtpk(s0[0], s0[1]); w[1] = cvtpk(s0[2], s0[3]); w[2] = cvtpk(s1[0], s1[1]); w[3] = cvtpk(s1[2], s1[3]);
        *(u32x4*)(out + (size_t)(rb * 128 + row) * (2 * DM) + br * DM + cb * 128 + ch * 8) = w;
    }
#undef K3_DMA
}

extern "C" void kernel_launch(void* const* d_in, const int* in_sizes, int n_in, void* d_out, int out_size, void* d_ws,
                              size_t ws_size, hipStream_t stream) {
    const unsigned short* xA = (const unsigned short*)d_in[0];
    const unsigned short* wqkvA = (const unsigned short*)d_in[1];
    const unsigned short* woA = (const unsigned short*)d_in[2];
    const unsigned short* xB = (const unsigned short*)d_in[3];
    const unsigned short* wqkvB = (const unsigned short*)d_in[4];
    const unsigned short* woB = (const unsigned short*)d_in[5];
    char* ws = (char*)d_ws;
    (void)hipFuncSetAttribute((const void*)qkv_kernel, hipFuncAttributeMaxDynamicSharedMemorySize, K1_LDS);
    (void)hipFuncSetAttribute((const void*)attn_kernel, hipFuncAttributeMaxDynamicSharedMemorySize, K2_LDS);
    (void)hipFuncSetAttribute((const void*)oproj_kernel, hipFuncAttributeMaxDynamicSharedMemorySize, K3_LDS);
    qkv_kernel<<<256, 512, K1_LDS, stream>>>(xA, wqkvA, xB, wqkvB, ws);
    attn_kernel<<<256, 512, K2_LDS, stream>>>(ws);
    oproj_kernel<<<256, 512, K3_LDS, stream>>>(woA, woB, ws, (unsigned short*)d_out);
}
```

```cpp
#include <hip/hip_runtime.h>
#include <hip/hip_bf16.h>

typedef __attribute__((ext_vector_type(8))) short bf16x8;
typedef __attribute__((ext_vector_type(4))) short s16x4;
typedef __attribute__((ext_vector_type(16))) float f32x16;
typedef __attribute__((ext_vector_type(4))) float f32x4;
typedef __attribute__((ext_vector_type(2))) float f32x2;
typedef __attribute__((ext_vector_type(4))) unsigned u32x4;
typedef __attribute__((address_space(3))) char lds_char;
#define LDSP(T, p) ((__attribute__((address_space(3))) T*)(p))

#define DI __device__ __forceinline__
#define MFMA32(a, b, c) __builtin_amdgcn_mfma_f32_32x32x16_bf16((a), (b), (c), 0, 0, 0)
#define MFMA16(a, b, c) __builtin_amdgcn_mfma_f32_16x16x32_bf16((a), (b), (c), 0, 0, 0)
#define SBAR() __builtin_amdgcn_sched_barrier(0)
#define PIN(x) asm volatile("" : "+v"(x))
#define WAIT_BAR(N) asm volatile("s_waitcnt vmcnt(" #N ") lgkmcnt(0)\n\ts_barrier" ::: "memory")

#define STAMP_DECL
#define STAMP(i) do {} while (0)
#define STAMP_FLUSH(kid) do {} while (0)
constexpr int SEQ = 2048, DM = 1024, NH = 32;
constexpr float LOG2E = 1.4426950408889634f;
constexpr float QSCALE = 0.17677669529663687f * LOG2E;

constexpr size_t WS_Q = 0;
constexpr size_t WS_KV = (size_t)4 << 20;
constexpr size_t WS_PQ = (size_t)12 << 20;
constexpr size_t WS_O = (size_t)16 << 20;
constexpr size_t WS_KVP = (size_t)24 << 20;
constexpr size_t WS_ZP = (size_t)25 << 20;

typedef __attribute__((ext_vector_type(2))) __bf16 bf16x2_t;
DI unsigned cvtpk(float lo, float hi) {
    return __builtin_bit_cast(unsigned, __builtin_convertvector((f32x2{lo, hi}), bf16x2_t));
}
DI bf16x8 pack8(const f32x16& x, int s) {
    u32x4 p;
    p[0] = cvtpk(x[8 * s + 0], x[8 * s + 1]);
    p[1] = cvtpk(x[8 * s + 2], x[8 * s + 3]);
    p[2] = cvtpk(x[8 * s + 4], x[8 * s + 5]);
    p[3] = cvtpk(x[8 * s + 6], x[8 * s + 7]);
    return __builtin_bit_cast(bf16x8, p);
}
DI void dma16(__amdgpu_buffer_rsrc_t rsrc, unsigned voff, unsigned soff, unsigned lds_dst) {
    unsigned keep;
    asm volatile("s_mov_b32 %0, m0\n\ts_mov_b32 m0, %4\n\ts_nop 0\n\tbuffer_load_dwordx4 %1, %2, %3 offen lds\n\ts_mov_b32 m0, %0"
                 : "=&s"(keep) : "v"(voff), "s"(rsrc), "s"(soff), "s"(lds_dst) : "memory");
}
DI void dma16_lo(__amdgpu_buffer_rsrc_t rsrc, unsigned voff, unsigned soff, unsigned lds_dst) {
    unsigned keep;
    asm volatile("s_mov_b32 %0, m0\n\ts_mov_b32 m0, %4\n\ts_mov_b32 exec_hi, 0\n\tbuffer_load_dwordx4 %1, %2, %3 offen lds\n\ts_mov_b32 exec_hi, -1\n\ts_mov_b32 m0, %0"
                 : "=&s"(keep) : "v"(voff), "s"(rsrc), "s"(soff), "s"(lds_dst) : "memory");
}
DI __amdgpu_buffer_rsrc_t mk_rsrc(const void* p, unsigned bytes) {
    return __builtin_amdgcn_make_buffer_rsrc((void*)p, (short)0, (int)bytes, 0x00020000);
}
#define PREFETCH_WO 1
#define TAIL_BAR 1
#define K3_DEPTH 3
#define K2V 2
#define ST_AUX 0
DI void st16(__amdgpu_buffer_rsrc_t rsrc, unsigned voff, u32x4 v) { __builtin_amdgcn_raw_buffer_store_b128(v, rsrc, voff, 0, ST_AUX); }
DI void st16(__amdgpu_buffer_rsrc_t rsrc, unsigned voff, bf16x8 v) { st16(rsrc, voff, __builtin_bit_cast(u32x4, v)); }
#define ASM_LD16(dst, rsrc, voff, soff) asm volatile("buffer_load_dwordx4 %0, %1, %2, %3 offen" : "=v"(dst) : "v"(voff), "s"(rsrc), "s"(soff) : "memory")
#define ASM_LD8(dst, rsrc, voff, soff) asm volatile("buffer_load_dwordx2 %0, %1, %2, %3 offen" : "=v"(dst) : "v"(voff), "s"(rsrc), "s"(soff) : "memory")
DI bf16x8 lds_rd128(const lds_char* p) { return *LDSP(const bf16x8, p); }
DI s16x4 lds_tr(const lds_char* p) {
    typedef short v4i16_t __attribute__((ext_vector_type(4)));
    return __builtin_bit_cast(s16x4, __builtin_amdgcn_ds_read_tr16_b64_v4i16(LDSP(v4i16_t, p)));
}
DI bf16x8 cat8(s16x4 lo, s16x4 hi) { return __builtin_shufflevector(lo, hi, 0, 1, 2, 3, 4, 5, 6, 7); }
DI unsigned rfl(unsigned x) { return __builtin_amdgcn_readfirstlane(x); }

constexpr int K1_ASLOT = 32768, K1_BSLOT = 12288, K1_BOFF = 3 * K1_ASLOT, K1_LDS = K1_BOFF + 5 * K1_BSLOT;

struct K1Frag { bf16x8 a[2]; bf16x8 b[3]; };

__global__ __launch_bounds__(768) void qkv_kernel(const unsigned short* __restrict__ xA, const unsigned short* __restrict__ wA,
                                                  const unsigned short* __restrict__ xB, const unsigned short* __restrict__ wB,
                                                  char* __restrict__ ws) {
    extern __shared__ __attribute__((aligned(16))) char smem[];
    const lds_char* lds = (const lds_char*)smem;
    const unsigned lds0 = (unsigned)(size_t)lds;
    const int tid = threadIdx.x, lane = tid & 63;
    const int wid = rfl(tid >> 6), wm = wid & 3, hh = (wid >> 2) & 1;
    const int r32 = lane & 31, hi = lane >> 5;
    STAMP_DECL; STAMP(0);
    const int xg = blockIdx.x & 7, j = blockIdx.x >> 3;
    const int br = xg >> 2;
    const int mb = (xg & 1) * 4 + (j & 3);
    const int hp = ((xg >> 1) & 1) * 8 + (j >> 2);
    const unsigned short* X = br ? xB : xA;
    const unsigned short* W = br ? wB : wA;
    const __amdgpu_buffer_rsrc_t rX = mk_rsrc(X + (size_t)mb * 256 * DM, 256 * DM * 2);
    const __amdgpu_buffer_rsrc_t rW = mk_rsrc(W, DM * 3 * DM * 2);
#define NEXT_A(s) ((s) == 2 * K1_ASLOT ? 0u : (s) + K1_ASLOT)
#define NEXT_B(s) ((s) == 4 * K1_BSLOT ? 0u : (s) + K1_BSLOT)

    if (wid >= 8) {
        const int hw = wid - 8;
        unsigned voA[2], voB[3];
#pragma unroll
        for (int i = 0; i < 2; ++i) {
            const int R = hw * 64 + i * 8 + (lane >> 3);
            voA[i] = R * (DM * 2) + ((lane & 7) ^ ((R >> 1) & 7)) * 16;
        }
#pragma unroll
        for (int i = 0; i < 3; ++i) {
            const int blk = hw * 12 + i * 4 + (lane >> 4), kg = blk / 6, cb = blk % 6;
            voB[i] = (kg * 4 + ((lane >> 2) & 3)) * (3 * DM * 2) + ((cb >> 1) * DM + hp * 64 + (cb & 1) * 32 + (lane & 3) * 8) * 2;
        }
        const unsigned dA = lds0 + hw * 8192, dB = lds0 + K1_BOFF + hw * 3072;
#define K1_DMA_A(t, slot) do { _Pragma("unroll") for (int i_ = 0; i_ < 8; ++i_) \
            dma16(rX, voA[i_ & 1], (unsigned)(t) * 128u + (i_ >> 1) * (16u * DM * 2), dA + (slot) + i_ * 1024); } while (0)
#define K1_DMA_B(u, slot) do { _Pragma("unroll") for (int i_ = 0; i_ < 3; ++i_) \
            dma16(rW, voB[i_], (unsigned)(u) * (32u * 3 * DM * 2), dB + (slot) + i_ * 1024); } while (0)
        K1_DMA_B(0, 0); K1_DMA_A(0, 0);
        K1_DMA_B(1, K1_BSLOT);
        WAIT_BAR(3);
        K1_DMA_B(2, 2 * K1_BSLOT); K1_DMA_A(1, K1_ASLOT);
        K1_DMA_B(3, 3 * K1_BSLOT);
        WAIT_BAR(14);
        K1_DMA_B(4, 4 * K1_BSLOT); K1_DMA_A(2, 2 * K1_ASLOT);
        K1_DMA_B(5, 0);
        WAIT_BAR(17);
        unsigned sa = 0, sb = K1_BSLOT;
#pragma unroll 1
        for (int t = 0; t < 13; ++t) {
            K1_DMA_B(2 * t + 6, sb); K1_DMA_A(t + 3, sa);
            sb = NEXT_B(sb); sa = NEXT_A(sa);
            WAIT_BAR(25);
            K1_DMA_B(2 * t + 7, sb);
            sb = NEXT_B(sb);
            WAIT_BAR(17);
        }
        WAIT_BAR(14);
        WAIT_BAR(3);
        WAIT_BAR(0);
        return;
    }

    unsigned rdA[4];
#pragma unroll
    for (int s = 0; s < 4; ++s) rdA[s] = (wm * 64 + r32) * 128 + (((2 * s + hi) ^ ((r32 >> 1) & 7)) * 16);
    const unsigned rdB = K1_BOFF + hi * (2 * 1536) + ((lane & 15) >> 2) * 64 + ((lane >> 4) & 1) * 32 + (lane & 3) * 8 + hh * 256;

    f32x16 acc[2][3];
    {
        const float init = br ? 1e-6f : 0.f;
#pragma unroll
        for (int i = 0; i < 2; ++i)
#pragma unroll
            for (int r = 0; r < 16; ++r) { acc[i][0][r] = init; acc[i][1][r] = init; acc[i][2][r] = 0.f; }
    }
    K1Frag F0, F1;
#define RDA(F, i, ks, sa) F.a[i] = lds_rd128(lds + (sa) + rdA[ks] + (i) * 4096)
#define RDB(F, c, sp, sb) F.b[c] = cat8(lds_tr(lds + (sb) + rdB + (sp) * 6144 + (c) * 512), lds_tr(lds + (sb) + rdB + (sp) * 6144 + 1536 + (c) * 512))
#define MM(F, i, c) do { \
        if ((c) == 0) acc[i][c] = MFMA32(F.b[c], F.a[i], acc[i][c]); \
        else if ((c) == 2) acc[i][c] = MFMA32(F.a[i], F.b[c], acc[i][c]); \
        else { if (BR) acc[i][c] = MFMA32(F.a[i], F.b[c], acc[i][c]); else acc[i][c] = MFMA32(F.b[c], F.a[i], acc[i][c]); } } while (0)
#define STEP(FC, FN, ks, sa, sp, sb) do { \
        MM(FC, 0, 0); SBAR(); RDA(FN, 0, ks, sa); RDB(FN, 0, sp, sb); SBAR(); \
        MM(FC, 1, 0); SBAR(); RDA(FN, 1, ks, sa); RDB(FN, 1, sp, sb); SBAR(); \
        MM(FC, 0, 1); SBAR(); RDB(FN, 2, sp, sb); SBAR(); \
        MM(FC, 1, 1); MM(FC, 0, 2); MM(FC, 1, 2); SBAR(); } while (0)
#define STEP_LAST(FC) do { MM(FC, 0, 0); MM(FC, 1, 0); MM(FC, 0, 1); MM(FC, 1, 1); MM(FC, 0, 2); MM(FC, 1, 2); } while (0)
#define LBAR() do { asm volatile("s_waitcnt lgkmcnt(0)\n\ts_barrier" ::: "memory"); SBAR(); } while (0)

    auto mainloop = [&](auto brc) {
        constexpr bool BR = decltype(brc)::value;
        STAMP(1);
        LBAR();
        STAMP(2);
        RDA(F0, 0, 0, 0); RDB(F0, 0, 0, 0); RDA(F0, 1, 0, 0); RDB(F0, 1, 0, 0); RDB(F0, 2, 0, 0);
        unsigned sa = 0, sb = 0;
#pragma unroll 1
        for (int t = 0; t < 15; ++t) {
            const unsigned sb1 = NEXT_B(sb), sb2 = NEXT_B(sb1), sa1 = NEXT_A(sa);
            STEP(F0, F1, 1, sa, 1, sb);
            LBAR();
            STEP(F1, F0, 2, sa, 0, sb1);
            STEP(F0, F1, 3, sa, 1, sb1);
            LBAR();
            STEP(F1, F0, 0, sa1, 0, sb2);
            sa = sa1; sb = sb2;
        }
        {
            STEP(F0, F1, 1, 0, 1, 0);
            LBAR();
            STEP(F1, F0, 2, 0, 0, K1_BSLOT);
            STEP(F0, F1, 3, 0, 1, K1_BSLOT);
            STEP_LAST(F1);
        }
    };
    if (br) mainloop(std::integral_constant<bool, true>{}); else mainloop(std::integral_constant<bool, false>{});

    STAMP(3);
    const int head = hp * 2 + hh;
    const int tile0 = mb * 8 + wm * 2;
    const __amdgpu_buffer_rsrc_t rWS = mk_rsrc(ws, 26u << 20);
    if (!br) {
#pragma unroll
        for (int i = 0; i < 2; ++i) {
            const int tl = head * 64 + tile0 + i;
            f32x16 q = acc[i][0] * QSCALE;
#pragma unroll
            for (int s = 0; s < 2; ++s) {
                st16(rWS, WS_KV + ((tl * 4 + s) * 64 + lane) * 16, pack8(acc[i][1], s));
                st16(rWS, WS_KV + ((tl * 4 + 2 + s) * 64 + lane) * 16, pack8(acc[i][2], s));
                st16(rWS, WS_Q + ((tl * 2 + s) * 64 + lane) * 16, pack8(q, s));
            }
        }
    } else {
        auto phi = [](f32x16& v) {
#pragma unroll
            for (int r = 0; r < 16; r += 2) {
                const f32x2 t = {v[r], v[r + 1]};
                const f32x2 u = t * LOG2E + (-1e-6f * LOG2E);
                const f32x2 e = {__builtin_amdgcn_fmed3f(__builtin_amdgcn_exp2f(u[0]), 0.f, 1.f), __builtin_amdgcn_fmed3f(__builtin_amdgcn_exp2f(u[1]), 0.f, 1.f)};
                const f32x2 m = {__int_as_float(max(__float_as_int(t[0]), 0x358637bd)), __int_as_float(max(__float_as_int(t[1]), 0x358637bd))};
                const f32x2 o2 = m + e;
                v[r] = o2[0]; v[r + 1] = o2[1];
            }
        };
        f32x16 kv = {}, zz = {};
        bf16x8 ones;
#pragma unroll
        for (int e = 0; e < 8; ++e) ones[e] = (short)0x3F80;
#pragma unroll
        for (int i = 0; i < 2; ++i) {
            phi(acc[i][1]);
#pragma unroll
            for (int s = 0; s < 2; ++s) {
                const bf16x8 pk = pack8(acc[i][1], s), vv = pack8(acc[i][2], s);
                kv = MFMA32(pk, vv, kv);
                zz = MFMA32(pk, ones, zz);
            }
        }
        phi(acc[0][0]);
#pragma unroll
        for (int s = 0; s < 2; ++s) st16(rWS, WS_PQ + (((head * 64 + tile0) * 2 + s) * 64 + lane) * 16, pack8(acc[0][0], s));
        lds_char* red = (lds_char*)smem + K1_ASLOT;
#pragma unroll
        for (int p = 0; p < 8; ++p) *LDSP(f32x2, red + wid * 4096 + p * 512 + lane * 8) = f32x2{kv[2 * p], kv[2 * p + 1]};
        if (r32 == 0) {
#pragma unroll
            for (int g = 0; g < 4; ++g) *LDSP(f32x4, red + 32768 + wid * 128 + hi * 64 + g * 16) = f32x4{zz[4 * g], zz[4 * g + 1], zz[4 * g + 2], zz[4 * g + 3]};
        }
        phi(acc[1][0]);
#pragma unroll
        for (int s = 0; s < 2; ++s) st16(rWS, WS_PQ + (((head * 64 + tile0 + 1) * 2 + s) * 64 + lane) * 16, pack8(acc[1][0], s));
        asm volatile("s_waitcnt lgkmcnt(0)\n\ts_barrier" ::: "memory");
        {
            const int h2 = tid >> 8, idx = tid & 255;
            f32x4 s4 = *LDSP(const f32x4, red + (h2 * 4 + 0) * 4096 + idx * 16);
#pragma unroll
            for (int m = 1; m < 4; ++m) s4 += *LDSP(const f32x4, red + (h2 * 4 + m) * 4096 + idx * 16);
            st16(rWS, WS_KVP + (((hp * 2 + h2) * 8 + mb) * 256 + idx) * 16, __builtin_bit_cast(u32x4, s4));
            if (tid < 64) {
                const int h3 = tid >> 5, i3 = tid & 31;
                float z = 0.f;
#pragma unroll
                for (int m = 0; m < 4; ++m) z += *LDSP(const float, red + 32768 + (h3 * 4 + m) * 128 + i3 * 4);
                ((float*)(ws + WS_ZP))[((hp * 2 + h3) * 8 + mb) * 32 + i3] = z;
            }
        }
    }
    STAMP_FLUSH(0);
#undef RDA
#undef RDB
#undef MM
#undef STEP
#undef STEP_LAST
#undef LBAR
}

constexpr int K2_SLOT = 16384, K2_BKV = 4 * K2_SLOT, K2_BZ = K2_BKV + 32768, K2_BPQ = K2_BZ + 1024, K2_XOFF = K2_BPQ + 16384,
              K2_LDS = K2_XOFF + 4096 + 1024;

DI float xhalf_max(float m) {
    auto rr = __builtin_amdgcn_permlane32_swap(__float_as_uint(m), __float_as_uint(m), false, false);
    return __builtin_fmaxf(__uint_as_float(rr[0]), __uint_as_float(rr[1]));
}
DI float xhalf_sum(float m) {
    auto rr = __builtin_amdgcn_permlane32_swap(__float_as_uint(m), __float_as_uint(m), false, false);
    return __uint_as_float(rr[0]) + __uint_as_float(rr[1]);
}
DI float max16(const f32x16& s) {
    float a = __builtin_fmaxf(s[0], s[1]);
#pragma unroll
    for (int r = 2; r < 16; ++r) a = __builtin_fmaxf(a, s[r]);
    return a;
}

constexpr int k2_rest(int k) { return ((PREFETCH_WO && k >= 5 && k <= 12) ? 2 : 0) + (k == 4 ? 4 : 0); }
constexpr int k2_nops(int k) { return (k + 3 < 16 ? 4 : 0) + k2_rest(k); }
constexpr int k2_wait(int k) { return k <= 1 ? 21 : k2_rest(k - 2) + k2_nops(k - 1) + k2_nops(k); }

__global__ __launch_bounds__(768) void attn_kernel(char* __restrict__ ws, const unsigned short* __restrict__ woA,
                                                   const unsigned short* __restrict__ woB) {
    extern __shared__ __attribute__((aligned(16))) char smem[];
    const lds_char* lds = (const lds_char*)smem;
    const unsigned lds0 = (unsigned)(size_t)lds;
    const int tid = threadIdx.x, lane = tid & 63;
    const int wid = rfl(tid >> 6);
    const int hi = lane >> 5;
    STAMP_DECL; STAMP(0);
    const int xg = blockIdx.x & 7, j0 = blockIdx.x >> 3;
    const int head = xg * 4 + (j0 >> 3), qb = j0 & 7;
    const __amdgpu_buffer_rsrc_t rWS = mk_rsrc(ws, 26u << 20);
    const __amdgpu_buffer_rsrc_t rKV = mk_rsrc(ws + WS_KV + (size_t)head * 64 * 4096, 64 * 4096);
    const unsigned vl16 = lane * 16;
#define BF(x) __builtin_bit_cast(bf16x8, x)

    if (wid >= 8) {
        const int hw = wid - 8;
        const unsigned vo = ((hw >> 1) * 32 + (hw & 1)) * 4096 + vl16, dKV = lds0 + hw * 4096;
#define K2_DMA(st) do { const unsigned d_ = dKV + ((st) & 3) * K2_SLOT; \
            dma16(rKV, vo, (unsigned)(st) * 8192u + 4096u, d_); dma16(rKV, vo, (unsigned)(st) * 8192u + 4096u + 1024u, d_ + 1024); \
            dma16(rKV, vo, (unsigned)(st) * 8192u + 4096u + 2048u, d_ + 2048); dma16(rKV, vo, (unsigned)(st) * 8192u + 3072u, d_ + 3072); } while (0)
        const __amdgpu_buffer_rsrc_t rW = mk_rsrc((const char*)((xg >> 2) ? woB : woA) + j0 * 65536 + hw * 16384, 16384);
        K2_DMA(0); K2_DMA(1);
        WAIT_BAR(4);
#pragma unroll
        for (int k = 0; k < 15; ++k) {
            if (k == 0) {
                K2_DMA(2);
#pragma unroll
                for (int i = 0; i < 8; ++i) dma16(rWS, vl16, (unsigned)(WS_KVP + (head * 8 + hw * 2) * 4096 + i * 1024), lds0 + K2_BKV + hw * 8192 + i * 1024);
                dma16(rWS, vl16, (unsigned)(WS_ZP + head * 1024), lds0 + K2_BZ);
#pragma unroll
                for (int i = 0; i < 4; ++i) dma16(rWS, vl16, (unsigned)(WS_PQ + (head * 64 + qb * 8 + hw * 2) * 2048 + i * 1024), lds0 + K2_BPQ + hw * 4096 + i * 1024);
            }
            if (k + 3 < 16) K2_DMA(k + 3);
            if (PREFETCH_WO && k >= 5 && k <= 12) {
                dma16(rW, vl16, (unsigned)((k - 5) * 2048), lds0 + K2_XOFF + 4096);
                dma16(rW, vl16, (unsigned)((k - 5) * 2048 + 1024), lds0 + K2_XOFF + 4096);
            }
            if (k == 3) {
#pragma unroll
                for (int u = 0; u < 2; ++u) {
                    const int pr = hw * 2 + u;
                    f32x2 a = *LDSP(const f32x2, lds + K2_BKV + pr * 512 + lane * 8), z = *LDSP(const f32x2, lds + K2_BZ + hi * 64 + pr * 8);
#pragma unroll
                    for (int m = 1; m < 8; ++m) {
                        a += *LDSP(const f32x2, lds + K2_BKV + m * 4096 + pr * 512 + lane * 8);
                        z += *LDSP(const f32x2, lds + K2_BZ + m * 128 + hi * 64 + pr * 8);
                    }
                    lds_char* xs = (lds_char*)smem + K2_XOFF + (pr >> 2) * 1024 + vl16 + (pr & 3) * 4;
                    *LDSP(unsigned, xs) = cvtpk(a[0], a[1]);
                    *LDSP(unsigned, xs + 2048) = cvtpk(z[0], z[1]);
                }
            }
            if (k == 4) {
                const lds_char* xs = lds + K2_XOFF + vl16;
                const bf16x8 kv0 = lds_rd128(xs), kv1 = lds_rd128(xs + 1024), z0 = lds_rd128(xs + 2048), z1 = lds_rd128(xs + 3072);
#pragma unroll
                for (int u = 0; u < 2; ++u) {
                    const lds_char* pp = lds + K2_BPQ + (hw * 2 + u) * 2048 + vl16;
                    const bf16x8 pq0 = lds_rd128(pp), pq1 = lds_rd128(pp + 1024);
                    f32x16 num = {}, den = {};
                    num = MFMA32(kv0, pq0, num); den = MFMA32(z0, pq0, den);
                    num = MFMA32(kv1, pq1, num); den = MFMA32(z1, pq1, den);
                    const float rd = 1.0f / (den[0] + 1e-6f);
                    num *= rd;
                    const unsigned ob = WS_O + ((64 + qb * 8 + hw * 2 + u) * 32 + head) * 2048 + vl16;
                    st16(rWS, ob, pack8(num, 0));
                    st16(rWS, ob + 1024, pack8(num, 1));
                }
            }
            asm volatile("s_waitcnt vmcnt(%0) lgkmcnt(0)\n\ts_barrier" :: "i"(k2_wait(k)) : "memory");
        }
        return;
    }

    const int qp = wid & 3, kh = wid >> 2;
    bf16x8 qa0, qa1, qb0, qb1, kx0, kx1, ky0, ky1, v0, v1;
    {
        const unsigned sq = WS_Q + (head * 64 + qb * 8 + qp * 2) * 2048;
        ASM_LD16(qa0, rWS, vl16, sq); ASM_LD16(qa1, rWS, vl16, sq + 1024);
        ASM_LD16(qb0, rWS, vl16, sq + 2048); ASM_LD16(qb1, rWS, vl16, sq + 3072);
        ASM_LD16(ky0, rKV, vl16, 0u); ASM_LD16(ky1, rKV, vl16, 1024u);
        const unsigned sk = kh * 32 * 4096;
        ASM_LD16(kx0, rKV, vl16, sk); ASM_LD16(kx1, rKV, vl16, sk + 1024);
        ASM_LD16(v0, rKV, vl16, sk + 2048);
    }
    bf16x8 sel;
    {
        const short v = (((lane >> 4) & 1) == (((lane & 15) >> 2) & 1)) ? (short)0x3F80 : (short)0;
#pragma unroll
        for (int e = 0; e < 8; ++e) sel[e] = v;
    }
    const lds_char* fb = lds + kh * 8192 + vl16;
    f32x16 sA, sB, negm, oA = {}, oB = {};
    f32x4 lA = {}, lB = {};
    u32x4 p0, p1 = {};
    STAMP(1);
    asm volatile("s_waitcnt vmcnt(0)" : "+v"(qa0), "+v"(qa1), "+v"(qb0), "+v"(qb1), "+v"(kx0), "+v"(kx1), "+v"(ky0), "+v"(ky1), "+v"(v0) :: "memory");
    STAMP(2);
    {
        f32x16 z16 = {};
        sA = MFMA32(ky0, qa0, z16); sB = MFMA32(ky0, qb0, z16);
        sA = MFMA32(ky1, qa1, sA); sB = MFMA32(ky1, qb1, sB);
        const float m = xhalf_max(__builtin_fmaxf(max16(sA), max16(sB)));
#pragma unroll
        for (int r = 0; r < 16; ++r) negm[r] = -m;
    }
    PIN(negm);
    sA = MFMA32(kx0, qa0, negm); sA = MFMA32(kx1, qa1, sA);
    v1 = v0;

#define EX(X, i) X[i] = __builtin_amdgcn_exp2f(X[i])
#define CV(P, n, X, i) P[n] = cvtpk(X[i], X[(i) + 1])
#define ATT(SC, SN, K0, K1, Q0, Q1, OP, LP, OC, LC, R1, R2, R3, R6) do { \
        SN = MFMA32(K0, Q0, negm); \
        SBAR(); R1; EX(SC, 0); EX(SC, 1); EX(SC, 2); CV(p0, 0, SC, 0); SBAR(); \
        OP = MFMA32(v1, BF(p1), OP); \
        SBAR(); R2; EX(SC, 3); EX(SC, 4); CV(p0, 1, SC, 2); EX(SC, 5); SBAR(); \
        SN = MFMA32(K1, Q1, SN); \
        SBAR(); R3; EX(SC, 6); CV(p0, 2, SC, 4); EX(SC, 7); EX(SC, 8); SBAR(); \
        LP = MFMA16(sel, BF(p1), LP); \
        SBAR(); CV(p0, 3, SC, 6); SBAR(); EX(SC, 9); EX(SC, 10); SBAR(); \
        OC = MFMA32(v0, BF(p0), OC); \
        SBAR(); CV(p1, 0, SC, 8); EX(SC, 11); EX(SC, 12); CV(p1, 1, SC, 10); EX(SC, 13); SBAR(); \
        LC = MFMA16(sel, BF(p0), LC); \
        SBAR(); R6; EX(SC, 14); EX(SC, 15); CV(p1, 2, SC, 12); CV(p1, 3, SC, 14); SBAR(); \
        } while (0)
#define TILE2(KC0, KC1, KN0, KN1, TP) do { \
        ATT(sA, sB, KC0, KC1, qb0, qb1, oB, lB, oA, lA, KN0 = lds_rd128(TP), KN1 = lds_rd128((TP) + 1024), v1 = lds_rd128((TP) + 3072), (void)0); \
        ATT(sB, sA, KN0, KN1, qa0, qa1, oA, lA, oB, lB, (void)0, (void)0, (void)0, v0 = lds_rd128((TP) + 2048)); } while (0)

    STAMP(3);
#pragma unroll 1
    for (int k = 0; k < 16; ++k) {
        asm volatile("s_waitcnt lgkmcnt(0)\n\ts_barrier" ::: "memory");
        SBAR();
        const lds_char* cur = fb + (k & 3) * K2_SLOT;
        TILE2(kx0, kx1, ky0, ky1, cur);
        TILE2(ky0, ky1, kx0, kx1, cur + 4096);
    }
    oB = MFMA32(v1, BF(p1), oB);
    lB = MFMA16(sel, BF(p1), lB);
    STAMP(4);
    f32x16 o; float l;
    bf16x8 q0, q1;
    {
        const f32x16 og = kh ? oA : oB;
        const float lg = kh ? lA[0] : lB[0];
        o = kh ? oB : oA; l = kh ? lB[0] : lA[0];
        q0 = kh ? qb0 : qa0; q1 = kh ? qb1 : qa1;
        lds_char* mw = (lds_char*)smem + K2_BKV + wid * 5120 + vl16;
#pragma unroll
        for (int g = 0; g < 4; ++g) *LDSP(f32x4, mw + g * 1024) = f32x4{og[4 * g], og[4 * g + 1], og[4 * g + 2], og[4 * g + 3]};
        *LDSP(float, mw + 4096) = lg;
        asm volatile("s_waitcnt lgkmcnt(0)\n\ts_barrier" ::: "memory");
        const lds_char* mr = lds + K2_BKV + (wid ^ 4) * 5120 + vl16;
#pragma unroll
        for (int g = 0; g < 4; ++g) {
            const f32x4 t = *LDSP(const f32x4, mr + g * 1024);
            o[4 * g] += t[0]; o[4 * g + 1] += t[1]; o[4 * g + 2] += t[2]; o[4 * g + 3] += t[3];
        }
        l += *LDSP(const float, mr + 4096);
    }
    const int qt = qb * 8 + qp * 2 + kh;
    {
        float t = 0.f;
#pragma unroll
        for (int r = 0; r < 16; ++r) t = __builtin_fmaxf(t, __builtin_fabsf(o[r]));
        const bool nan_o = [&] { bool b = false;
#pragma unroll
            for (int r = 0; r < 16; ++r) b |= (o[r] != o[r]);
            return b; }();
        const bool bad = !(l < 1.8446744e19f) || !(l > 5.4210109e-20f) || !(t < 3.0e38f) || nan_o;
        if (__builtin_expect(__any(bad), 0)) {
            const u32x4* KV = (const u32x4*)(ws + WS_KV) + (size_t)head * 64 * 256 + lane;
            float m = -INFINITY, ls = 0.f;
#pragma unroll
            for (int r = 0; r < 16; ++r) o[r] = 0.f;
            for (int jt = 0; jt < 64; ++jt) {
                const bf16x8 a0 = BF(KV[jt * 256]), a1 = BF(KV[jt * 256 + 64]), b0 = BF(KV[jt * 256 + 128]), b1 = BF(KV[jt * 256 + 192]);
                f32x16 s = {};
                s = MFMA32(a0, q0, s); s = MFMA32(a1, q1, s);
                const float mn = __builtin_fmaxf(m, xhalf_max(max16(s)));
                const float al = __builtin_amdgcn_exp2f(m - mn);
                m = mn; ls *= al;
#pragma unroll
                for (int r = 0; r < 16; ++r) { o[r] *= al; s[r] = __builtin_amdgcn_exp2f(s[r] - mn); ls += s[r]; }
                o = MFMA32(b0, pack8(s, 0), o); o = MFMA32(b1, pack8(s, 1), o);
            }
            l = xhalf_sum(ls);
        }
    }
    {
        const float rl = 1.0f / l;
        o *= rl;
        const unsigned ob = WS_O + (qt * 32 + head) * 2048 + vl16;
        st16(rWS, ob, pack8(o, 0));
        st16(rWS, ob + 1024, pack8(o, 1));
    }
    STAMP_FLUSH(1);
#undef EX
#undef ATT
#undef CV
#undef TILE2
#undef K2_DMA
}

constexpr int K3_SLOT = 16384, K3_RP = 528, K3_LDS = 2 * 128 * K3_RP;

__global__ __launch_bounds__(512, 2) void oproj_kernel(const unsigned short* __restrict__ woA, const unsigned short* __restrict__ woB,
                                                       const char* __restrict__ ws, unsigned short* __restrict__ out) {
    extern __shared__ __attribute__((aligned(16))) char smem[];
    const lds_char* lds = (const lds_char*)smem;
    const unsigned lds0 = (unsigned)(size_t)lds;
    const int tid = threadIdx.x, lane = tid & 63;
    const int wid = rfl(tid >> 6), rt = wid & 3, kg = wid >> 2;
    const int r32 = lane & 31, hi = lane >> 5;
    STAMP_DECL; STAMP(0);
    const int xg = blockIdx.x & 7, j = blockIdx.x >> 3;
    const int br = xg >> 2, rb = (xg & 3) * 4 + (j >> 3), cb = j & 7;
    const unsigned short* Wo = br ? woB : woA;
    const __amdgpu_buffer_rsrc_t rW = mk_rsrc(Wo, DM * DM * 2);
    const __amdgpu_buffer_rsrc_t rO = mk_rsrc(ws + WS_O + (size_t)(br * 64 + rb * 4 + rt) * 65536 + kg * 32768, 32768);
    const unsigned voW = (kg * 512 + rt * 8 + ((lane >> 2) & 3)) * (DM * 2) + (cb * 128 + (lane >> 4) * 32 + (lane & 3) * 8) * 2;
    const unsigned voW2 = voW + 4 * DM * 2;
    const unsigned dW = lds0 + kg * 8192 + rt * 2048;
    const unsigned vl16 = lane * 16;
#define K3_DMA(i) do { dma16(rW, voW, (unsigned)(i) * (32u * DM * 2), dW + ((i) & 7) * K3_SLOT); dma16(rW, voW2, (unsigned)(i) * (32u * DM * 2), dW + ((i) & 7) * K3_SLOT + 1024); } while (0)
    bf16x8 of[32];
#define K3_ISSUE(i) do { K3_DMA(i); ASM_LD16(of[2 * (i)], rO, vl16, (unsigned)((i) * 2048)); ASM_LD16(of[2 * (i) + 1], rO, vl16, (unsigned)((i) * 2048 + 1024)); } while (0)
#pragma unroll
    for (int i = 0; i < K3_DEPTH; ++i) K3_ISSUE(i);
    const unsigned rdW = kg * 8192 + hi * 1024 + ((lane & 15) >> 2) * 64 + ((lane >> 4) & 1) * 32 + (lane & 3) * 8;
    f32x16 acc[4] = {};
    bf16x8 w0[4], w1[4];
    STAMP(1);
#define K3_RD(Wf, c, i, s) Wf[c] = cat8(lds_tr(lds + ((i) & 7) * K3_SLOT + rdW + (s) * 4096 + (c) * 256), lds_tr(lds + ((i) & 7) * K3_SLOT + rdW + (s) * 4096 + 2048 + (c) * 256))
#define K3_WAIT(i, N) asm volatile("s_waitcnt vmcnt(%2) lgkmcnt(0)\n\ts_barrier" : "+v"(of[2 * (i)]), "+v"(of[2 * (i) + 1]) : "i"(N) : "memory")
    K3_WAIT(0, 4 * (K3_DEPTH - 1));
    STAMP(2);
    K3_RD(w0, 0, 0, 0); K3_RD(w0, 1, 0, 0); K3_RD(w0, 2, 0, 0); K3_RD(w0, 3, 0, 0);
#pragma unroll
    for (int i = 0; i < 16; ++i) {
        acc[0] = MFMA32(w0[0], of[2 * i], acc[0]); SBAR(); K3_RD(w1, 0, i, 1); K3_RD(w1, 1, i, 1); SBAR();
        acc[1] = MFMA32(w0[1], of[2 * i], acc[1]); SBAR(); K3_RD(w1, 2, i, 1); K3_RD(w1, 3, i, 1); SBAR();
        acc[2] = MFMA32(w0[2], of[2 * i], acc[2]); SBAR();
        if (i + K3_DEPTH < 16) K3_ISSUE(i + K3_DEPTH < 16 ? i + K3_DEPTH : 0);
        SBAR();
        acc[3] = MFMA32(w0[3], of[2 * i], acc[3]); SBAR();
        if (i < 15) {
            const int n = i + 1;
            const int last = (i + K3_DEPTH < 15) ? i + K3_DEPTH : 15;
            const int allowed = 4 * (last - n);
            K3_WAIT(n < 16 ? n : 15, allowed); SBAR();
            acc[0] = MFMA32(w1[0], of[2 * i + 1], acc[0]); SBAR(); K3_RD(w0, 0, n, 0); K3_RD(w0, 1, n, 0); SBAR();
            acc[1] = MFMA32(w1[1], of[2 * i + 1], acc[1]); SBAR(); K3_RD(w0, 2, n, 0); K3_RD(w0, 3, n, 0); SBAR();
            acc[2] = MFMA32(w1[2], of[2 * i + 1], acc[2]); SBAR();
            acc[3] = MFMA32(w1[3], of[2 * i + 1], acc[3]); SBAR();
        } else {
#pragma unroll
            for (int c = 0; c < 4; ++c) acc[c] = MFMA32(w1[c], of[2 * i + 1], acc[c]);
        }
    }
    asm volatile("s_waitcnt lgkmcnt(0)\n\ts_barrier" ::: "memory");
    STAMP(3);
    {
        lds_char* rw = (lds_char*)smem + (kg * 128 + rt * 32 + r32) * K3_RP + hi * 16;
#pragma unroll
        for (int c = 0; c < 4; ++c)
#pragma unroll
            for (int g = 0; g < 4; ++g)
                *LDSP(f32x4, rw + c * 128 + g * 32) = f32x4{acc[c][4 * g], acc[c][4 * g + 1], acc[c][4 * g + 2], acc[c][4 * g + 3]};
    }
    asm volatile("s_waitcnt lgkmcnt(0)\n\ts_barrier" ::: "memory");
#pragma unroll
    for (int it = 0; it < 4; ++it) {
        const int row = it * 32 + (tid >> 4), ch = tid & 15;
        const lds_char* p = lds + row * K3_RP + ch * 32;
        const f32x4 a0 = *LDSP(const f32x4, p), a1 = *LDSP(const f32x4, p + 16);
        const f32x4 b0 = *LDSP(const f32x4, p + 128 * K3_RP), b1 = *LDSP(const f32x4, p + 128 * K3_RP + 16);
        const f32x4 s0 = a0 + b0, s1 = a1 + b1;
        u32x4 w;
        w[0] = cvtpk(s0[0], s0[1]); w[1] = cvtpk(s0[2], s0[3]); w[2] = cvtpk(s1[0], s1[1]); w[3] = cvtpk(s1[2], s1[3]);
        *(u32x4*)(out + (size_t)(rb * 128 + row) * (2 * DM) + br * DM + cb * 128 + ch * 8) = w;
    }
    STAMP_FLUSH(2);
#undef K3_DMA
#undef K3_RD
#undef K3_ISSUE
#undef K3_WAIT
}

extern "C" void kernel_launch(void* const* d_in, const int* in_sizes, int n_in, void* d_out, int out_size, void* d_ws,
                              size_t ws_size, hipStream_t stream) {
    const unsigned short* xA = (const unsigned short*)d_in[0];
    const unsigned short* wqkvA = (const unsigned short*)d_in[1];
    const unsigned short* woA = (const unsigned short*)d_in[2];
    const unsigned short* xB = (const unsigned short*)d_in[3];
    const unsigned short* wqkvB = (const unsigned short*)d_in[4];
    const unsigned short* woB = (const unsigned short*)d_in[5];
    char* ws = (char*)d_ws;
    (void)hipFuncSetAttribute((const void*)qkv_kernel, hipFuncAttributeMaxDynamicSharedMemorySize, K1_LDS);
    (void)hipFuncSetAttribute((const void*)attn_kernel, hipFuncAttributeMaxDynamicSharedMemorySize, K2_LDS);
    (void)hipFuncSetAttribute((const void*)oproj_kernel, hipFuncAttributeMaxDynamicSharedMemorySize, K3_LDS);
    qkv_kernel<<<256, 768, K1_LDS, stream>>>(xA, wqkvA, xB, wqkvB, ws);
    attn_kernel<<<256, 768, K2_LDS, stream>>>(ws, woA, woB);
    oproj_kernel<<<256, 512, K3_LDS, stream>>>(woA, woB, ws, (unsigned short*)d_out);
}
```

```cpp
#include <hip/hip_runtime.h>
#include <hip/hip_bf16.h>

typedef __attribute__((ext_vector_type(8))) short bf16x8;
typedef __attribute__((ext_vector_type(4))) short s16x4;
typedef __attribute__((ext_vector_type(16))) float f32x16;
typedef __attribute__((ext_vector_type(4))) float f32x4;
typedef __attribute__((ext_vector_type(2))) float f32x2;
typedef __attribute__((ext_vector_type(4))) unsigned u32x4;
typedef __attribute__((address_space(3))) char lds_char;
#define LDSP(T, p) ((__attribute__((address_space(3))) T*)(p))

#define DI __device__ __forceinline__
#define MFMA32(a, b, c) __builtin_amdgcn_mfma_f32_32x32x16_bf16((a), (b), (c), 0, 0, 0)
#define MFMA16(a, b, c) __builtin_amdgcn_mfma_f32_16x16x32_bf16((a), (b), (c), 0, 0, 0)
#define SBAR() __builtin_amdgcn_sched_barrier(0)
#define PIN(x) asm volatile("" : "+v"(x))
#define WAIT_BAR(N) asm volatile("s_waitcnt vmcnt(" #N ") lgkmcnt(0)\n\ts_barrier" ::: "memory")

#define STAMP_DECL
#define STAMP(i) do {} while (0)
#define STAMP_FLUSH(kid) do {} while (0)
constexpr int SEQ = 2048, DM = 1024, NH = 32;
constexpr float LOG2E = 1.4426950408889634f;
constexpr float QSCALE = 0.17677669529663687f * LOG2E;

constexpr size_t WS_Q = 0;
constexpr size_t WS_KV = (size_t)4 << 20;
constexpr size_t WS_PQ = (size_t)12 << 20;
constexpr size_t WS_O = (size_t)16 << 20;
constexpr size_t WS_KVP = (size_t)24 << 20;
constexpr size_t WS_ZP = (size_t)25 << 20;

typedef __attribute__((ext_vector_type(2))) __bf16 bf16x2_t;
DI unsigned cvtpk(float lo, float hi) {
    return __builtin_bit_cast(unsigned, __builtin_convertvector((f32x2{lo, hi}), bf16x2_t));
}
DI bf16x8 pack8(const f32x16& x, int s) {
    u32x4 p;
    p[0] = cvtpk(x[8 * s + 0], x[8 * s + 1]);
    p[1] = cvtpk(x[8 * s + 2], x[8 * s + 3]);
    p[2] = cvtpk(x[8 * s + 4], x[8 * s + 5]);
    p[3] = cvtpk(x[8 * s + 6], x[8 * s + 7]);
    return __builtin_bit_cast(bf16x8, p);
}
DI void dma16(__amdgpu_buffer_rsrc_t rsrc, unsigned voff, unsigned soff, unsigned lds_dst) {
    unsigned keep;
    asm volatile("s_mov_b32 %0, m0\n\ts_mov_b32 m0, %4\n\ts_nop 0\n\tbuffer_load_dwordx4 %1, %2, %3 offen lds\n\ts_mov_b32 m0, %0"
                 : "=&s"(keep) : "v"(voff), "s"(rsrc), "s"(soff), "s"(lds_dst) : "memory");
}
DI void dma16_lo(__amdgpu_buffer_rsrc_t rsrc, unsigned voff, unsigned soff, unsigned lds_dst) {
    unsigned keep;
    asm volatile("s_mov_b32 %0, m0\n\ts_mov_b32 m0, %4\n\ts_mov_b32 exec_hi, 0\n\tbuffer_load_dwordx4 %1, %2, %3 offen lds\n\ts_mov_b32 exec_hi, -1\n\ts_mov_b32 m0, %0"
                 : "=&s"(keep) : "v"(voff), "s"(rsrc), "s"(soff), "s"(lds_dst) : "memory");
}
DI __amdgpu_buffer_rsrc_t mk_rsrc(const void* p, unsigned bytes) {
    return __builtin_amdgcn_make_buffer_rsrc((void*)p, (short)0, (int)bytes, 0x00020000);
}
#define PREFETCH_WO 1
#define K3_ROT 0
#define AUX_OUT 2
#define AUX_A 16
#define AUX_B 16
#define AUX_O 16
#define AUX_OB 16
template <int AUX> DI void st16(__amdgpu_buffer_rsrc_t rsrc, unsigned voff, u32x4 v) { __builtin_amdgcn_raw_buffer_store_b128(v, rsrc, voff, 0, AUX); }
template <int AUX> DI void st16(__amdgpu_buffer_rsrc_t rsrc, unsigned voff, bf16x8 v) { st16<AUX>(rsrc, voff, __builtin_bit_cast(u32x4, v)); }
#define ASM_LD16(dst, rsrc, voff, soff) asm volatile("buffer_load_dwordx4 %0, %1, %2, %3 offen" : "=v"(dst) : "v"(voff), "s"(rsrc), "s"(soff) : "memory")
#define ASM_LD8(dst, rsrc, voff, soff) asm volatile("buffer_load_dwordx2 %0, %1, %2, %3 offen" : "=v"(dst) : "v"(voff), "s"(rsrc), "s"(soff) : "memory")
DI bf16x8 lds_rd128(const lds_char* p) { return *LDSP(const bf16x8, p); }
DI s16x4 lds_tr(const lds_char* p) {
    typedef short v4i16_t __attribute__((ext_vector_type(4)));
    return __builtin_bit_cast(s16x4, __builtin_amdgcn_ds_read_tr16_b64_v4i16(LDSP(v4i16_t, p)));
}
DI bf16x8 cat8(s16x4 lo, s16x4 hi) { return __builtin_shufflevector(lo, hi, 0, 1, 2, 3, 4, 5, 6, 7); }
DI unsigned rfl(unsigned x) { return __builtin_amdgcn_readfirstlane(x); }

constexpr int K1_ASLOT = 32768, K1_BSLOT = 12288, K1_BOFF = 3 * K1_ASLOT, K1_LDS = K1_BOFF + 5 * K1_BSLOT;

DI int k1_head(int br, int hp, int sec, int hh) {
    const int off = sec == 0 ? 0 : (sec == 1 || br) ? 3 : 6;
    return ((hp & 8) + ((hp + off) & 7)) * 2 + hh;
}
DI u32x4 pack44(const f32x4& a, const f32x4& b) {
    u32x4 p;
    p[0] = cvtpk(a[0], a[1]); p[1] = cvtpk(a[2], a[3]); p[2] = cvtpk(b[0], b[1]); p[3] = cvtpk(b[2], b[3]);
    return p;
}

__global__ __launch_bounds__(768) void qkv_kernel(const unsigned short* __restrict__ xA, const unsigned short* __restrict__ wA,
                                                  const unsigned short* __restrict__ xB, const unsigned short* __restrict__ wB,
                                                  char* __restrict__ ws) {
    extern __shared__ __attribute__((aligned(16))) char smem[];
    const lds_char* lds = (const lds_char*)smem;
    const unsigned lds0 = (unsigned)(size_t)lds;
    const int tid = threadIdx.x, lane = tid & 63;
    const int wid = rfl(tid >> 6), wm = wid & 3, hh = (wid >> 2) & 1;
    STAMP_DECL; STAMP(0);
    const int xg = blockIdx.x & 7, j = blockIdx.x >> 3;
    const int br = xg >> 2;
    const int mb = (xg & 1) * 4 + (j & 3);
    const int hp = ((xg >> 1) & 1) * 8 + (j >> 2);
#define NEXT_A(s) ((s) == 2 * K1_ASLOT ? 0u : (s) + K1_ASLOT)
#define NEXT_B(s) ((s) == 4 * K1_BSLOT ? 0u : (s) + K1_BSLOT)

    if (wid >= 8) {
        const unsigned short* X = br ? xB : xA;
        const unsigned short* W = br ? wB : wA;
        const __amdgpu_buffer_rsrc_t rX = mk_rsrc(X + (size_t)mb * 256 * DM, 256 * DM * 2);
        const __amdgpu_buffer_rsrc_t rW = mk_rsrc(W, DM * 3 * DM * 2);
        const int hw = wid - 8;
        unsigned voA[2], voB[3];
#pragma unroll
        for (int i = 0; i < 2; ++i) {
            const int R = hw * 64 + i * 8 + (lane >> 3);
            voA[i] = R * (DM * 2) + ((lane & 7) ^ ((R >> 1) & 7)) * 16;
        }
#pragma unroll
        for (int i = 0; i < 3; ++i) {
            const int blk = i * 4 + (lane >> 4), kg = hw * 2 + blk / 6, cb = blk % 6;
            const int hd = k1_head(br, hp, cb >> 1, cb & 1);
            voB[i] = (kg * 4 + ((lane >> 2) & 3)) * (3 * DM * 2) + ((cb >> 1) * DM + hd * 32 + ((lane & 3) ^ ((hw & 1) * 2)) * 8) * 2;
        }
        const unsigned dA = lds0 + hw * 8192, dB = lds0 + K1_BOFF + hw * 3072;
#define K1_DMA_A(t, slot) do { _Pragma("unroll") for (int i_ = 0; i_ < 8; ++i_) \
            dma16(rX, voA[i_ & 1], (unsigned)(t) * 128u + (i_ >> 1) * (16u * DM * 2), dA + (slot) + i_ * 1024); } while (0)
#define K1_DMA_B(u, slot) do { _Pragma("unroll") for (int i_ = 0; i_ < 3; ++i_) \
            dma16(rW, voB[i_], (unsigned)(u) * (32u * 3 * DM * 2), dB + (slot) + i_ * 1024); } while (0)
        K1_DMA_B(0, 0); K1_DMA_A(0, 0);
        K1_DMA_B(1, K1_BSLOT);
        WAIT_BAR(3);
        K1_DMA_B(2, 2 * K1_BSLOT); K1_DMA_A(1, K1_ASLOT);
        K1_DMA_B(3, 3 * K1_BSLOT);
        WAIT_BAR(14);
        K1_DMA_B(4, 4 * K1_BSLOT); K1_DMA_A(2, 2 * K1_ASLOT);
        K1_DMA_B(5, 0);
        WAIT_BAR(17);
        unsigned sa = 0, sb = K1_BSLOT;
#pragma unroll 1
        for (int t = 0; t < 13; ++t) {
            K1_DMA_B(2 * t + 6, sb); K1_DMA_A(t + 3, sa);
            sb = NEXT_B(sb); sa = NEXT_A(sa);
            WAIT_BAR(25);
            K1_DMA_B(2 * t + 7, sb);
            sb = NEXT_B(sb);
            WAIT_BAR(17);
        }
        WAIT_BAR(14);
        WAIT_BAR(3);
        WAIT_BAR(0);
        return;
    }

    const int c16 = lane & 15, g = lane >> 4;
    unsigned rdA[2], rdB[2];
#pragma unroll
    for (int u = 0; u < 2; ++u) {
        rdA[u] = (wm * 64 + c16) * 128 + (((4 * u + g) ^ ((c16 >> 1) & 7)) * 16);
        rdB[u] = K1_BOFF + g * 3072 + (c16 >> 2) * 64 + ((u ^ (g & 1)) * 32) + (lane & 3) * 8 + hh * 256;
    }
    f32x4 acc[4][6];
    {
        const float init = br ? 1e-6f : 0.f;
#pragma unroll
        for (int i = 0; i < 4; ++i)
#pragma unroll
            for (int c = 0; c < 6; ++c) { const float v = c < 4 ? init : 0.f; acc[i][c] = f32x4{v, v, v, v}; }
    }
    bf16x8 a0[4], a1[4], bb[6];
#define RDA(A, i, u, sa) A[i] = lds_rd128(lds + (sa) + rdA[u] + (i) * 2048)
#define RDB(c, sb) bb[c] = cat8(lds_tr(lds + (sb) + rdB[(c) & 1] + ((c) >> 1) * 512), lds_tr(lds + (sb) + rdB[(c) & 1] + ((c) >> 1) * 512 + 1536))
#define MM(A, i, c) do { \
        if ((c) < 2 || ((c) < 4 && !BR)) acc[i][c] = MFMA16(bb[c], A[i], acc[i][c]); \
        else acc[i][c] = MFMA16(A[i], bb[c], acc[i][c]); } while (0)
#define MM2(A, i, c) do { MM(A, i, c); MM(A, (i) + 1, c); } while (0)
#define PH_A(A, sb) do { \
        MM2(A, 0, 0); SBAR(); RDB(3, sb); SBAR(); MM2(A, 2, 0); SBAR(); RDB(4, sb); SBAR(); \
        MM2(A, 0, 1); SBAR(); RDB(5, sb); SBAR(); MM2(A, 2, 1); MM2(A, 0, 2); MM2(A, 2, 2); SBAR(); } while (0)
#define PH_B(A, AN, un, san, sbn) do { \
        MM2(A, 0, 3); SBAR(); RDB(0, sbn); RDA(AN, 0, un, san); SBAR(); MM2(A, 2, 3); SBAR(); RDA(AN, 1, un, san); RDA(AN, 2, un, san); SBAR(); \
        MM2(A, 0, 4); SBAR(); RDA(AN, 3, un, san); RDB(1, sbn); SBAR(); MM2(A, 2, 4); SBAR(); RDB(2, sbn); SBAR(); \
        MM2(A, 0, 5); MM2(A, 2, 5); SBAR(); } while (0)
#define PH_B_LAST(A) do { MM2(A, 0, 3); MM2(A, 2, 3); MM2(A, 0, 4); MM2(A, 2, 4); MM2(A, 0, 5); MM2(A, 2, 5); } while (0)
#define LBAR() do { asm volatile("s_waitcnt lgkmcnt(0)\n\ts_barrier" ::: "memory"); SBAR(); } while (0)

    auto mainloop = [&](auto brc) {
        constexpr bool BR = decltype(brc)::value;
        STAMP(1);
        LBAR();
        STAMP(2);
        RDB(0, 0); RDA(a0, 0, 0, 0); RDA(a0, 1, 0, 0); RDA(a0, 2, 0, 0); RDA(a0, 3, 0, 0); RDB(1, 0); RDB(2, 0);
        unsigned sa = 0, sb = 0;
#pragma unroll 1
        for (int t = 0; t < 15; ++t) {
            const unsigned sb1 = NEXT_B(sb), sb2 = NEXT_B(sb1), sa1 = NEXT_A(sa);
            PH_A(a0, sb);
            LBAR();
            PH_B(a0, a1, 1, sa, sb1);
            PH_A(a1, sb1);
            LBAR();
            PH_B(a1, a0, 0, sa1, sb2);
            sa = sa1; sb = sb2;
        }
        {
            PH_A(a0, 0);
            LBAR();
            PH_B(a0, a1, 1, 0, K1_BSLOT);
            PH_A(a1, K1_BSLOT);
            PH_B_LAST(a1);
        }
    int tz = threadIdx.x; asm volatile("" : "+v"(tz));
    const int tid = tz, c16 = tz & 15, g = (tz >> 4) & 3;
    STAMP(3);
    const int tile0 = mb * 8 + wm * 2;
    const unsigned po = ((g >> 1) * 64 + (g & 1) * 32) * 16;
    const __amdgpu_buffer_rsrc_t rWS = mk_rsrc(ws, 26u << 20);
    const int hq = k1_head(br, hp, 0, hh), hk = k1_head(br, hp, 1, hh);
    if constexpr (!BR) {
        const int hv = k1_head(0, hp, 2, hh);
#pragma unroll
        for (int m = 0; m < 2; ++m) {
#pragma unroll
            for (int a = 0; a < 2; ++a) {
                st16<AUX_A>(rWS, WS_KV + (hk * 64 + tile0 + m) * 4096 + po + ((c16 & 7) | (a << 3) | ((c16 >> 3) << 4)) * 16,
                     pack44(acc[2 * m + a][2], acc[2 * m + a][3]));
                st16<AUX_A>(rWS, WS_KV + (hv * 64 + tile0 + m) * 4096 + 2048 + po + (16 * a + c16) * 16, pack44(acc[2 * m][4 + a], acc[2 * m + 1][4 + a]));
                st16<AUX_A>(rWS, WS_Q + (hq * 64 + tile0 + m) * 2048 + po + (16 * a + c16) * 16,
                     pack44(acc[2 * m + a][0] * QSCALE, acc[2 * m + a][1] * QSCALE));
            }
        }
    } else {
        auto phi = [](f32x4& v) {
#pragma unroll
            for (int r = 0; r < 4; r += 2) {
                const f32x2 t = {v[r], v[r + 1]};
                const f32x2 u = t * LOG2E + (-1e-6f * LOG2E);
                const f32x2 e = {__builtin_amdgcn_fmed3f(__builtin_amdgcn_exp2f(u[0]), 0.f, 1.f), __builtin_amdgcn_fmed3f(__builtin_amdgcn_exp2f(u[1]), 0.f, 1.f)};
                const f32x2 mx = {__int_as_float(max(__float_as_int(t[0]), 0x358637bd)), __int_as_float(max(__float_as_int(t[1]), 0x358637bd))};
                const f32x2 o2 = mx + e;
                v[r] = o2[0]; v[r + 1] = o2[1];
            }
        };
        f32x4 kvt[2][2] = {}, zt[2] = {};
        bf16x8 ones;
#pragma unroll
        for (int e = 0; e < 8; ++e) ones[e] = (short)0x3F80;
#pragma unroll
        for (int m = 0; m < 2; ++m) {
            bf16x8 vf[2];
#pragma unroll
            for (int b = 0; b < 2; ++b) vf[b] = __builtin_bit_cast(bf16x8, pack44(acc[2 * m][4 + b], acc[2 * m + 1][4 + b]));
#pragma unroll
            for (int b = 0; b < 2; ++b) {
                phi(acc[2 * m][2 + b]); phi(acc[2 * m + 1][2 + b]);
                const bf16x8 pk = __builtin_bit_cast(bf16x8, pack44(acc[2 * m][2 + b], acc[2 * m + 1][2 + b]));
                kvt[b][0] = MFMA16(pk, vf[0], kvt[b][0]);
                kvt[b][1] = MFMA16(pk, vf[1], kvt[b][1]);
                zt[b] = MFMA16(pk, ones, zt[b]);
            }
        }
#pragma unroll
        for (int i = 0; i < 2; ++i) {
            phi(acc[i][0]); phi(acc[i][1]);
            st16<AUX_B>(rWS, WS_PQ + (hq * 64 + tile0) * 2048 + po + (16 * i + c16) * 16, pack44(acc[i][0], acc[i][1]));
        }
        lds_char* red = (lds_char*)smem + K1_ASLOT;
#pragma unroll
        for (int e = 0; e < 2; ++e) {
            lds_char* rp = red + wid * 4096 + (g >> 1) * 2048 + ((g & 1) * 32 + 16 * e + c16) * 8;
            *LDSP(f32x2, rp) = f32x2{kvt[0][e][0], kvt[0][e][1]};
            *LDSP(f32x2, rp + 512) = f32x2{kvt[0][e][2], kvt[0][e][3]};
            *LDSP(f32x2, rp + 1024) = f32x2{kvt[1][e][0], kvt[1][e][1]};
            *LDSP(f32x2, rp + 1536) = f32x2{kvt[1][e][2], kvt[1][e][3]};
        }
        if (c16 == 0) {
            lds_char* zp = red + 32768 + wid * 128 + (g & 1) * 64 + (g >> 1) * 32;
            *LDSP(f32x4, zp) = zt[0];
            *LDSP(f32x4, zp + 16) = zt[1];
        }
#pragma unroll
        for (int i = 2; i < 4; ++i) {
            phi(acc[i][0]); phi(acc[i][1]);
            st16<AUX_B>(rWS, WS_PQ + (hq * 64 + tile0 + 1) * 2048 + po + (16 * (i - 2) + c16) * 16, pack44(acc[i][0], acc[i][1]));
        }
        asm volatile("s_waitcnt lgkmcnt(0)\n\ts_barrier" ::: "memory");
        {
            const int idx = tid & 255;
            f32x4 s4 = *LDSP(const f32x4, red + (hh * 4 + 0) * 4096 + idx * 16);
#pragma unroll
            for (int m = 1; m < 4; ++m) s4 += *LDSP(const f32x4, red + (hh * 4 + m) * 4096 + idx * 16);
            st16<AUX_B>(rWS, WS_KVP + ((hk * 8 + mb) * 256 + idx) * 16, __builtin_bit_cast(u32x4, s4));
            if (idx < 32) {
                float z = 0.f;
#pragma unroll
                for (int m = 0; m < 4; ++m) z += *LDSP(const float, red + 32768 + (hh * 4 + m) * 128 + idx * 4);
                ((float*)(ws + WS_ZP))[(hk * 8 + mb) * 32 + idx] = z;
            }
        }
    }
    };
    if (br) mainloop(std::integral_constant<bool, true>{}); else mainloop(std::integral_constant<bool, false>{});
    STAMP_FLUSH(0);
#undef RDA
#undef RDB
#undef MM
#undef MM2
#undef PH_A
#undef PH_B
#undef PH_B_LAST
#undef LBAR
#undef K1_DMA_A
#undef K1_DMA_B
}

constexpr int K2_SLOT = 16384, K2_XOFF = 8 * K2_SLOT, K2_LDS = K2_XOFF + 4096 + 1024;
constexpr unsigned K2_BARS = 0x1117;

DI float xhalf_max(float m) {
    auto rr = __builtin_amdgcn_permlane32_swap(__float_as_uint(m), __float_as_uint(m), false, false);
    return __builtin_fmaxf(__uint_as_float(rr[0]), __uint_as_float(rr[1]));
}
DI float xhalf_sum(float m) {
    auto rr = __builtin_amdgcn_permlane32_swap(__float_as_uint(m), __float_as_uint(m), false, false);
    return __uint_as_float(rr[0]) + __uint_as_float(rr[1]);
}
DI float max16(const f32x16& s) {
    float a = __builtin_fmaxf(s[0], s[1]);
#pragma unroll
    for (int r = 2; r < 16; ++r) a = __builtin_fmaxf(a, s[r]);
    return a;
}

__global__ __launch_bounds__(512, 2) void attn_kernel(char* __restrict__ ws, const unsigned short* __restrict__ woA,
                                                   const unsigned short* __restrict__ woB) {
    extern __shared__ __attribute__((aligned(16))) char smem[];
    const lds_char* lds = (const lds_char*)smem;
    const unsigned lds0 = (unsigned)(size_t)lds;
    const int tid = threadIdx.x, lane = tid & 63;
    const int wid = rfl(tid >> 6);
    const int hi = lane >> 5;
    STAMP_DECL; STAMP(0);
    const int xg = blockIdx.x & 7, j0 = blockIdx.x >> 3;
    const int head = xg * 4 + (j0 >> 3), qb = j0 & 7;
    const __amdgpu_buffer_rsrc_t rWS = mk_rsrc(ws, 26u << 20);
    const __amdgpu_buffer_rsrc_t rKV = mk_rsrc(ws + WS_KV + (size_t)head * 64 * 4096, 64 * 4096);
    const unsigned vl16 = lane * 16;
#define BF(x) __builtin_bit_cast(bf16x8, x)

    if (wid >= 4) {
        const int hw = wid - 4;
        const unsigned vo = hw * 4096 + vl16, dKV = lds0 + hw * 4096;
#define K2_DMA(st) do { const unsigned d_ = dKV + ((st) & 7) * K2_SLOT; \
            dma16(rKV, vo, (unsigned)(st) * K2_SLOT + 4096u, d_); dma16(rKV, vo, (unsigned)(st) * K2_SLOT + 4096u + 1024u, d_ + 1024); \
            dma16(rKV, vo, (unsigned)(st) * K2_SLOT + 4096u + 2048u, d_ + 2048); dma16(rKV, vo, (unsigned)(st) * K2_SLOT + 3072u, d_ + 3072); } while (0)
        K2_DMA(0); K2_DMA(1);
        WAIT_BAR(4);
        K2_DMA(2); K2_DMA(3);
        WAIT_BAR(8);
        K2_DMA(4); K2_DMA(5);
        WAIT_BAR(8);
        K2_DMA(6); K2_DMA(7);
        f32x2 kp[2][8];
        f32x4 zp[8];
        bf16x8 pq[4];
        {
            const unsigned vk = lane * 8, vz = hi * 64;
#pragma unroll
            for (int m = 0; m < 8; ++m) {
                ASM_LD8(kp[0][m], rWS, vk, (unsigned)(WS_KVP + (head * 8 + m) * 4096 + hw * 1024));
                ASM_LD8(kp[1][m], rWS, vk, (unsigned)(WS_KVP + (head * 8 + m) * 4096 + hw * 1024 + 512));
                ASM_LD16(zp[m], rWS, vz, (unsigned)(WS_ZP + (head * 8 + m) * 128 + hw * 16));
            }
#pragma unroll
            for (int i = 0; i < 4; ++i) ASM_LD16(pq[i], rWS, vl16, (unsigned)(WS_PQ + (head * 64 + qb * 8 + hw * 2) * 2048 + i * 1024));
        }
        WAIT_BAR(28);
        K2_DMA(8); K2_DMA(9); K2_DMA(10); K2_DMA(11);
        asm volatile("s_waitcnt vmcnt(16)"
                     : "+v"(kp[0][0]), "+v"(kp[0][1]), "+v"(kp[0][2]), "+v"(kp[0][3]), "+v"(kp[0][4]), "+v"(kp[0][5]), "+v"(kp[0][6]), "+v"(kp[0][7]),
                       "+v"(kp[1][0]), "+v"(kp[1][1]), "+v"(kp[1][2]), "+v"(kp[1][3]), "+v"(kp[1][4]), "+v"(kp[1][5]), "+v"(kp[1][6]), "+v"(kp[1][7]) :: "memory");
        asm volatile("" : "+v"(zp[0]), "+v"(zp[1]), "+v"(zp[2]), "+v"(zp[3]), "+v"(zp[4]), "+v"(zp[5]), "+v"(zp[6]), "+v"(zp[7]),
                          "+v"(pq[0]), "+v"(pq[1]), "+v"(pq[2]), "+v"(pq[3]));
        {
            f32x2 a0 = kp[0][0], a1 = kp[1][0];
            f32x4 z = zp[0];
#pragma unroll
            for (int m = 1; m < 8; ++m) { a0 += kp[0][m]; a1 += kp[1][m]; z += zp[m]; }
            typedef __attribute__((ext_vector_type(2))) unsigned u32x2;
            lds_char* xs = (lds_char*)smem + K2_XOFF + (hw >> 1) * 1024 + vl16 + (hw & 1) * 8;
            *LDSP(u32x2, xs) = u32x2{cvtpk(a0[0], a0[1]), cvtpk(a1[0], a1[1])};
            *LDSP(u32x2, xs + 2048) = u32x2{cvtpk(z[0], z[1]), cvtpk(z[2], z[3])};
        }
        WAIT_BAR(0);
        K2_DMA(12); K2_DMA(13); K2_DMA(14); K2_DMA(15);
        {
            const __amdgpu_buffer_rsrc_t rW = mk_rsrc((const char*)((xg >> 2) ? woB : woA) + j0 * 65536 + hw * 16384, 16384);
#pragma unroll
            for (int i = 0; i < 16; ++i) dma16(rW, vl16, (unsigned)(i * 1024), lds0 + K2_XOFF + 4096);
        }
        {
            const lds_char* xs = lds + K2_XOFF + vl16;
            const bf16x8 kv0 = lds_rd128(xs), kv1 = lds_rd128(xs + 1024), z0 = lds_rd128(xs + 2048), z1 = lds_rd128(xs + 3072);
#pragma unroll
            for (int u = 0; u < 2; ++u) {
                f32x16 num = {}, den = {};
                num = MFMA32(kv0, pq[2 * u], num); den = MFMA32(z0, pq[2 * u], den);
                num = MFMA32(kv1, pq[2 * u + 1], num); den = MFMA32(z1, pq[2 * u + 1], den);
                const float rd = 1.0f / (den[0] + 1e-6f);
                num *= rd;
                const unsigned ob = WS_O + ((64 + qb * 8 + hw * 2 + u) * 32 + head) * 2048 + vl16;
                st16<AUX_OB>(rWS, ob, pack8(num, 0));
                st16<AUX_OB>(rWS, ob + 1024, pack8(num, 1));
            }
        }
        asm volatile("s_waitcnt vmcnt(%0) lgkmcnt(0)\n\ts_barrier" :: "i"((PREFETCH_WO ? 16 : 0) + 4) : "memory");
        return;
    }

    const int qt0 = qb * 8 + wid * 2;
    bf16x8 qa0, qa1, qb0, qb1, kx0, kx1, ky0, ky1, v0, v1;
    {
        const unsigned sq = WS_Q + (head * 64 + qt0) * 2048;
        ASM_LD16(qa0, rWS, vl16, sq); ASM_LD16(qa1, rWS, vl16, sq + 1024);
        ASM_LD16(kx0, rKV, vl16, 0u); ASM_LD16(kx1, rKV, vl16, 1024u);
        ASM_LD16(qb0, rWS, vl16, sq + 2048); ASM_LD16(qb1, rWS, vl16, sq + 3072);
        ASM_LD16(v0, rKV, vl16, 2048u);
    }
    bf16x8 sel;
    {
        const short v = (((lane >> 4) & 1) == (((lane & 15) >> 2) & 1)) ? (short)0x3F80 : (short)0;
#pragma unroll
        for (int e = 0; e < 8; ++e) sel[e] = v;
    }
    const lds_char* fb = lds + vl16;
    f32x16 sA, sB, nmA, nmB, oA = {}, oB = {};
    f32x4 lA = {}, lB = {};
    u32x4 p0, p1 = {};
    STAMP(1);
    asm volatile("s_waitcnt vmcnt(0)" : "+v"(qa0), "+v"(qa1), "+v"(qb0), "+v"(qb1), "+v"(kx0), "+v"(kx1), "+v"(v0) :: "memory");
    STAMP(2);
    {
        f32x16 z16 = {};
        sA = MFMA32(kx0, qa0, z16); sB = MFMA32(kx0, qb0, z16);
        sA = MFMA32(kx1, qa1, sA); sB = MFMA32(kx1, qb1, sB);
        const float ma = xhalf_max(max16(sA)), mb = xhalf_max(max16(sB));
#pragma unroll
        for (int r = 0; r < 16; ++r) { nmA[r] = -ma; nmB[r] = -mb; sA[r] -= ma; }
    }
    PIN(nmA); PIN(nmB);
    v1 = v0;

#define EX(X, i) X[i] = __builtin_amdgcn_exp2f(X[i])
#define CV(P, n, X, i) P[n] = cvtpk(X[i], X[(i) + 1])
#define ATT(SC, SN, K0, K1, Q0, Q1, NM, OP, LP, OC, LC, R1, R2, R3, R6) do { \
        SN = MFMA32(K0, Q0, NM); \
        SBAR(); R1; EX(SC, 0); EX(SC, 1); EX(SC, 2); CV(p0, 0, SC, 0); SBAR(); \
        OP = MFMA32(v1, BF(p1), OP); \
        SBAR(); R2; EX(SC, 3); EX(SC, 4); CV(p0, 1, SC, 2); EX(SC, 5); SBAR(); \
        SN = MFMA32(K1, Q1, SN); \
        SBAR(); R3; EX(SC, 6); CV(p0, 2, SC, 4); EX(SC, 7); EX(SC, 8); SBAR(); \
        LP = MFMA16(sel, BF(p1), LP); \
        SBAR(); CV(p0, 3, SC, 6); SBAR(); EX(SC, 9); EX(SC, 10); SBAR(); \
        OC = MFMA32(v0, BF(p0), OC); \
        SBAR(); CV(p1, 0, SC, 8); EX(SC, 11); EX(SC, 12); CV(p1, 1, SC, 10); EX(SC, 13); SBAR(); \
        LC = MFMA16(sel, BF(p0), LC); \
        SBAR(); R6; EX(SC, 14); EX(SC, 15); CV(p1, 2, SC, 12); CV(p1, 3, SC, 14); SBAR(); \
        } while (0)
#define TILE2(KC0, KC1, KN0, KN1, TP) do { \
        ATT(sA, sB, KC0, KC1, qb0, qb1, nmB, oB, lB, oA, lA, KN0 = lds_rd128(TP), KN1 = lds_rd128((TP) + 1024), v1 = lds_rd128((TP) + 3072), (void)0); \
        ATT(sB, sA, KN0, KN1, qa0, qa1, nmA, oA, lA, oB, lB, (void)0, (void)0, (void)0, v0 = lds_rd128((TP) + 2048)); } while (0)

    STAMP(3);
#pragma unroll 1
    for (int k = 0; k < 16; ++k) {
        if ((K2_BARS >> k) & 1) asm volatile("s_waitcnt lgkmcnt(0)\n\ts_barrier" ::: "memory");
        SBAR();
        const lds_char* cur = fb + (k & 7) * K2_SLOT;
        TILE2(kx0, kx1, ky0, ky1, cur);
        TILE2(ky0, ky1, kx0, kx1, cur + 4096);
        TILE2(kx0, kx1, ky0, ky1, cur + 2 * 4096);
        TILE2(ky0, ky1, kx0, kx1, cur + 3 * 4096);
    }
    oB = MFMA32(v1, BF(p1), oB);
    lB = MFMA16(sel, BF(p1), lB);
    STAMP(4);
    auto finish = [&](f32x16 o, float l, const bf16x8 q0, const bf16x8 q1, int qt) {
        float t = 0.f;
        bool nan_o = false;
#pragma unroll
        for (int r = 0; r < 16; ++r) { t = __builtin_fmaxf(t, __builtin_fabsf(o[r])); nan_o |= (o[r] != o[r]); }
        const bool bad = !(l < 1.8446744e19f) || !(t < 3.0e38f) || nan_o;
        if (__builtin_expect(__any(bad), 0)) {
            const u32x4* KV = (const u32x4*)(ws + WS_KV) + (size_t)head * 64 * 256 + lane;
            float m = -INFINITY, ls = 0.f;
#pragma unroll
            for (int r = 0; r < 16; ++r) o[r] = 0.f;
#pragma unroll 1
            for (int jt = 0; jt < 64; ++jt) {
                const bf16x8 a0 = BF(KV[jt * 256]), a1 = BF(KV[jt * 256 + 64]), b0 = BF(KV[jt * 256 + 128]), b1 = BF(KV[jt * 256 + 192]);
                f32x16 sc = {};
                sc = MFMA32(a0, q0, sc); sc = MFMA32(a1, q1, sc);
                const float mn = __builtin_fmaxf(m, xhalf_max(max16(sc)));
                const float al = __builtin_amdgcn_exp2f(m - mn);
                m = mn; ls *= al;
#pragma unroll
                for (int r = 0; r < 16; ++r) { o[r] *= al; sc[r] = __builtin_amdgcn_exp2f(sc[r] - mn); ls += sc[r]; }
                o = MFMA32(b0, pack8(sc, 0), o); o = MFMA32(b1, pack8(sc, 1), o);
            }
            l = xhalf_sum(ls);
        }
        o *= 1.0f / l;
        const unsigned ob = WS_O + (qt * 32 + head) * 2048 + vl16;
        st16<AUX_O>(rWS, ob, pack8(o, 0));
        st16<AUX_O>(rWS, ob + 1024, pack8(o, 1));
    };
    finish(oA, lA[0], qa0, qa1, qt0);
    finish(oB, lB[0], qb0, qb1, qt0 + 1);
    STAMP_FLUSH(1);
#undef EX
#undef ATT
#undef CV
#undef TILE2
#undef K2_DMA
}

constexpr int K3_KG = 1032, K3_WOFF = 16384, K3_SLOT = K3_WOFF + 16 * K3_KG + 96  , K3_LDS = 4 * K3_SLOT;

__global__ __launch_bounds__(512, 2) void oproj_kernel(const unsigned short* __restrict__ woA, const unsigned short* __restrict__ woB,
                                                       const char* __restrict__ ws, unsigned short* __restrict__ out) {
    extern __shared__ __attribute__((aligned(16))) char smem[];
    const lds_char* lds = (const lds_char*)smem;
    const unsigned lds0 = (unsigned)(size_t)lds;
    const int tid = threadIdx.x, lane = tid & 63;
    const int wid = rfl(tid >> 6);
    STAMP_DECL; STAMP(0);
    const int xg = blockIdx.x & 7, j = blockIdx.x >> 3;
    const int br = xg >> 2, rb = (xg & 3) * 4 + (j >> 3), cb = j & 7;

    if (wid >= 4) {
        const int hw = wid - 4;
        const unsigned vl16 = lane * 16;
        const __amdgpu_buffer_rsrc_t rW = mk_rsrc(br ? woB : woA, DM * DM * 2);
        const __amdgpu_buffer_rsrc_t rO = mk_rsrc(ws + WS_O + (size_t)(br * 64 + rb * 4 + hw) * 65536, 65536);
        const unsigned voW = (hw * 16 + ((lane >> 2) & 3)) * (DM * 2) + (cb * 128 + (lane >> 4) * 32 + (lane & 3) * 8) * 2;
        const unsigned dO = lds0 + hw * 4096, dW = lds0 + K3_WOFF + hw * 4 * K3_KG;
        const unsigned rot = K3_ROT == 1 ? (j >> 3) * 4 : K3_ROT == 2 ? (j >> 3) * 4 + cb * 2 : K3_ROT == 3 ? cb * 2 : K3_ROT == 4 ? (j >> 3) + cb : 0;
#define K3_DMA(i) do { const unsigned sl_ = ((i) & 3) * K3_SLOT, k_ = ((unsigned)(i) + rot) & 15u; \
            _Pragma("unroll") for (int n_ = 0; n_ < 4; ++n_) { \
                dma16(rW, voW, k_ * (64u * DM * 2) + n_ * (4u * DM * 2), dW + sl_ + n_ * K3_KG); \
                dma16(rO, vl16, k_ * 4096u + n_ * 1024u, dO + sl_ + n_ * 1024); } } while (0)
        K3_DMA(0); K3_DMA(1);
        WAIT_BAR(8);
        K3_DMA(2);
        WAIT_BAR(8);
        K3_DMA(3);
#pragma unroll 1
        for (int i = 1; i < 13; ++i) {
            K3_DMA(i + 3);
            WAIT_BAR(16);
        }
        WAIT_BAR(8);
        WAIT_BAR(0);
        return;
    }

    const int wr = wid & 1, wc = wid >> 1, c16 = lane & 15, g = lane >> 4;
    const unsigned rdO = wr * 8192 + (g >> 1) * 1024 + ((g & 1) * 32 + c16) * 16;
    const unsigned rdW = K3_WOFF + ((g >> 1) * 4 + (g & 1)) * K3_KG + wc * 512 + (c16 >> 2) * 64 + (lane & 3) * 16;
    f32x4 acc[4][4] = {};
    bf16x8 o0[4], w0[4], o1[4], w1[4];
#define K3_RO(O, i, sl, hd) O[i] = lds_rd128(lds + (sl) + rdO + ((i) >> 1) * 4096 + ((i) & 1) * 256 + (hd) * 2048)
#define K3_RW(Wf, ct, sl, hd) Wf[ct] = cat8(lds_tr(lds + (sl) + rdW + (hd) * 8 * K3_KG + ((ct) >> 1) * 256 + ((ct) & 1) * 8), \
                                            lds_tr(lds + (sl) + rdW + (hd) * 8 * K3_KG + 2 * K3_KG + ((ct) >> 1) * 256 + ((ct) & 1) * 8))
#define K3_MM(OC, WC, i, ct) acc[i][ct] = MFMA16(WC[ct], OC[i], acc[i][ct])
#define K3_STEP(OC, WC, ON, WN, sl, hd) do { \
        K3_MM(OC, WC, 0, 0); K3_MM(OC, WC, 1, 0); SBAR(); K3_RW(WN, 0, sl, hd); K3_RO(ON, 0, sl, hd); SBAR(); \
        K3_MM(OC, WC, 2, 0); K3_MM(OC, WC, 3, 0); SBAR(); K3_RO(ON, 1, sl, hd); K3_RO(ON, 2, sl, hd); SBAR(); \
        K3_MM(OC, WC, 0, 1); K3_MM(OC, WC, 1, 1); SBAR(); K3_RO(ON, 3, sl, hd); K3_RW(WN, 1, sl, hd); SBAR(); \
        K3_MM(OC, WC, 2, 1); K3_MM(OC, WC, 3, 1); SBAR(); K3_RW(WN, 2, sl, hd); SBAR(); \
        K3_MM(OC, WC, 0, 2); K3_MM(OC, WC, 1, 2); SBAR(); K3_RW(WN, 3, sl, hd); SBAR(); \
        K3_MM(OC, WC, 2, 2); K3_MM(OC, WC, 3, 2); K3_MM(OC, WC, 0, 3); K3_MM(OC, WC, 1, 3); K3_MM(OC, WC, 2, 3); K3_MM(OC, WC, 3, 3); SBAR(); } while (0)
#define LBAR() do { asm volatile("s_waitcnt lgkmcnt(0)\n\ts_barrier" ::: "memory"); SBAR(); } while (0)
    STAMP(1);
    LBAR();
    STAMP(2);
    K3_RW(w0, 0, 0, 0); K3_RO(o0, 0, 0, 0); K3_RO(o0, 1, 0, 0); K3_RO(o0, 2, 0, 0); K3_RO(o0, 3, 0, 0); K3_RW(w0, 1, 0, 0); K3_RW(w0, 2, 0, 0); K3_RW(w0, 3, 0, 0);
#pragma unroll 1
    for (int i = 0; i < 15; ++i) {
        const unsigned sl = (i & 3) * K3_SLOT, sn = ((i + 1) & 3) * K3_SLOT;
        K3_STEP(o0, w0, o1, w1, sl, 1);
        LBAR();
        K3_STEP(o1, w1, o0, w0, sn, 0);
    }
    K3_STEP(o0, w0, o1, w1, 3 * K3_SLOT, 1);
#pragma unroll
    for (int i = 0; i < 4; ++i)
#pragma unroll
        for (int ct = 0; ct < 4; ++ct) K3_MM(o1, w1, i, ct);
    STAMP(3);
    {
        const __amdgpu_buffer_rsrc_t rOut = mk_rsrc(out, SEQ * 2 * DM * 2);
        const unsigned ob = ((rb * 128 + wr * 64 + c16) * (2 * DM) + br * DM + cb * 128 + wc * 64 + g * 8) * 2;
#pragma unroll
        for (int i = 0; i < 4; ++i)
#pragma unroll
            for (int u = 0; u < 2; ++u)
                st16<AUX_OUT>(rOut, ob + i * 16 * (2 * DM * 2) + u * 64, pack44(acc[i][2 * u], acc[i][2 * u + 1]));
    }
    STAMP_FLUSH(2);
#undef K3_DMA
#undef K3_RO
#undef K3_RW
#undef K3_MM
#undef K3_STEP
#undef LBAR
}

extern "C" void kernel_launch(void* const* d_in, const int* in_sizes, int n_in, void* d_out, int out_size, void* d_ws,
                              size_t ws_size, hipStream_t stream) {
    const unsigned short* xA = (const unsigned short*)d_in[0];
    const unsigned short* wqkvA = (const unsigned short*)d_in[1];
    const unsigned short* woA = (const unsigned short*)d_in[2];
    const unsigned short* xB = (const unsigned short*)d_in[3];
    const unsigned short* wqkvB = (const unsigned short*)d_in[4];
    const unsigned short* woB = (const unsigned short*)d_in[5];
    char* ws = (char*)d_ws;
    (void)hipFuncSetAttribute((const void*)qkv_kernel, hipFuncAttributeMaxDynamicSharedMemorySize, K1_LDS);
    (void)hipFuncSetAttribute((const void*)attn_kernel, hipFuncAttributeMaxDynamicSharedMemorySize, K2_LDS);
    (void)hipFuncSetAttribute((const void*)oproj_kernel, hipFuncAttributeMaxDynamicSharedMemorySize, K3_LDS);
    qkv_kernel<<<256, 768, K1_LDS, stream>>>(xA, wqkvA, xB, wqkvB, ws);
    attn_kernel<<<256, 512, K2_LDS, stream>>>(ws, woA, woB);
    oproj_kernel<<<256, 512, K3_LDS, stream>>>(woA, woB, ws, (unsigned short*)d_out);
}
```

```cpp
#include <hip/hip_runtime.h>
#include <hip/hip_bf16.h>

typedef __attribute__((ext_vector_type(8))) short bf16x8;
typedef __attribute__((ext_vector_type(4))) short s16x4;
typedef __attribute__((ext_vector_type(16))) float f32x16;
typedef __attribute__((ext_vector_type(4))) float f32x4;
typedef __attribute__((ext_vector_type(2))) float f32x2;
typedef __attribute__((ext_vector_type(4))) unsigned u32x4;
typedef __attribute__((address_space(3))) char lds_char;
#define LDSP(T, p) ((__attribute__((address_space(3))) T*)(p))

#define DI __device__ __forceinline__
#define MFMA32(a, b, c) __builtin_amdgcn_mfma_f32_32x32x16_bf16((a), (b), (c), 0, 0, 0)
#define MFMA16(a, b, c) __builtin_amdgcn_mfma_f32_16x16x32_bf16((a), (b), (c), 0, 0, 0)
#define SBAR() __builtin_amdgcn_sched_barrier(0)
#define PIN(x) asm volatile("" : "+v"(x))
#define WAIT_BAR(N) asm volatile("s_waitcnt vmcnt(" #N ") lgkmcnt(0)\n\ts_barrier" ::: "memory")

#define STAMP_DECL
#define STAMP(i) do {} while (0)
#define STAMP_FLUSH(kid) do {} while (0)
constexpr int SEQ = 2048, DM = 1024, NH = 32;
constexpr float LOG2E = 1.4426950408889634f;
constexpr float QSCALE = 0.17677669529663687f * LOG2E;

constexpr size_t WS_Q = 0;
constexpr size_t WS_KV = (size_t)4 << 20;
constexpr size_t WS_PQ = (size_t)12 << 20;
constexpr size_t WS_O = (size_t)16 << 20;
constexpr size_t WS_KVP = (size_t)24 << 20;
constexpr size_t WS_ZP = (size_t)25 << 20;
constexpr size_t WS_CNT = WS_ZP + 65536;

typedef __attribute__((ext_vector_type(2))) __bf16 bf16x2_t;
DI unsigned cvtpk(float lo, float hi) {
    return __builtin_bit_cast(unsigned, __builtin_convertvector((f32x2{lo, hi}), bf16x2_t));
}
DI bf16x8 pack8(const f32x16& x, int s) {
    u32x4 p;
    p[0] = cvtpk(x[8 * s + 0], x[8 * s + 1]);
    p[1] = cvtpk(x[8 * s + 2], x[8 * s + 3]);
    p[2] = cvtpk(x[8 * s + 4], x[8 * s + 5]);
    p[3] = cvtpk(x[8 * s + 6], x[8 * s + 7]);
    return __builtin_bit_cast(bf16x8, p);
}
DI void dma16(__amdgpu_buffer_rsrc_t rsrc, unsigned voff, unsigned soff, unsigned lds_dst) {
    unsigned keep;
    asm volatile("s_mov_b32 %0, m0\n\ts_mov_b32 m0, %4\n\ts_nop 0\n\tbuffer_load_dwordx4 %1, %2, %3 offen lds\n\ts_mov_b32 m0, %0"
                 : "=&s"(keep) : "v"(voff), "s"(rsrc), "s"(soff), "s"(lds_dst) : "memory");
}
DI void dma16_sc1(__amdgpu_buffer_rsrc_t rsrc, unsigned voff, unsigned soff, unsigned lds_dst) {
    unsigned keep;
    asm volatile("s_mov_b32 %0, m0\n\ts_mov_b32 m0, %4\n\ts_nop 0\n\tbuffer_load_dwordx4 %1, %2, %3 offen sc1 lds\n\ts_mov_b32 m0, %0"
                 : "=&s"(keep) : "v"(voff), "s"(rsrc), "s"(soff), "s"(lds_dst) : "memory");
}
DI void dma16_lo(__amdgpu_buffer_rsrc_t rsrc, unsigned voff, unsigned soff, unsigned lds_dst) {
    unsigned keep;
    asm volatile("s_mov_b32 %0, m0\n\ts_mov_b32 m0, %4\n\ts_mov_b32 exec_hi, 0\n\tbuffer_load_dwordx4 %1, %2, %3 offen lds\n\ts_mov_b32 exec_hi, -1\n\ts_mov_b32 m0, %0"
                 : "=&s"(keep) : "v"(voff), "s"(rsrc), "s"(soff), "s"(lds_dst) : "memory");
}
DI __amdgpu_buffer_rsrc_t mk_rsrc(const void* p, unsigned bytes) {
    return __builtin_amdgcn_make_buffer_rsrc((void*)p, (short)0, (int)bytes, 0x00020000);
}
#define PREFETCH_WO 1
#define K3_ROT 0
#define K3_IL 1
#define POLL_SLEEP 6
#define AUX_OUT 0
#define AUX_A 16
#define AUX_B 16
#define AUX_O 16
#define AUX_OB 16
template <int AUX> DI void st16(__amdgpu_buffer_rsrc_t rsrc, unsigned voff, u32x4 v) { __builtin_amdgcn_raw_buffer_store_b128(v, rsrc, voff, 0, AUX); }
template <int AUX> DI void st16(__amdgpu_buffer_rsrc_t rsrc, unsigned voff, bf16x8 v) { st16<AUX>(rsrc, voff, __builtin_bit_cast(u32x4, v)); }
#define ASM_LD16(dst, rsrc, voff, soff) asm volatile("buffer_load_dwordx4 %0, %1, %2, %3 offen" : "=v"(dst) : "v"(voff), "s"(rsrc), "s"(soff) : "memory")
#define ASM_LD8(dst, rsrc, voff, soff) asm volatile("buffer_load_dwordx2 %0, %1, %2, %3 offen" : "=v"(dst) : "v"(voff), "s"(rsrc), "s"(soff) : "memory")
DI bf16x8 lds_rd128(const lds_char* p) { return *LDSP(const bf16x8, p); }
DI s16x4 lds_tr(const lds_char* p) {
    typedef short v4i16_t __attribute__((ext_vector_type(4)));
    return __builtin_bit_cast(s16x4, __builtin_amdgcn_ds_read_tr16_b64_v4i16(LDSP(v4i16_t, p)));
}
DI bf16x8 cat8(s16x4 lo, s16x4 hi) { return __builtin_shufflevector(lo, hi, 0, 1, 2, 3, 4, 5, 6, 7); }
DI unsigned rfl(unsigned x) { return __builtin_amdgcn_readfirstlane(x); }

constexpr int K1_ASLOT = 32768, K1_BSLOT = 12288, K1_BOFF = 3 * K1_ASLOT, K1_LDS = K1_BOFF + 5 * K1_BSLOT;

DI int k1_head(int br, int hp, int sec, int hh) {
    const int off = sec == 0 ? 0 : (sec == 1 || br) ? 3 : 6;
    return ((hp & 8) + ((hp + off) & 7)) * 2 + hh;
}
DI u32x4 pack44(const f32x4& a, const f32x4& b) {
    u32x4 p;
    p[0] = cvtpk(a[0], a[1]); p[1] = cvtpk(a[2], a[3]); p[2] = cvtpk(b[0], b[1]); p[3] = cvtpk(b[2], b[3]);
    return p;
}

__global__ __launch_bounds__(768) void qkv_kernel(const unsigned short* __restrict__ xA, const unsigned short* __restrict__ wA,
                                                  const unsigned short* __restrict__ xB, const unsigned short* __restrict__ wB,
                                                  char* __restrict__ ws) {
    extern __shared__ __attribute__((aligned(16))) char smem[];
    const lds_char* lds = (const lds_char*)smem;
    const unsigned lds0 = (unsigned)(size_t)lds;
    const int tid = threadIdx.x, lane = tid & 63;
    const int wid = rfl(tid >> 6), wm = wid & 3, hh = (wid >> 2) & 1;
    STAMP_DECL; STAMP(0);
    const int xg = blockIdx.x & 7, j = blockIdx.x >> 3;
    const int br = xg >> 2;
    const int mb = (xg & 1) * 4 + (j & 3);
    const int hp = ((xg >> 1) & 1) * 8 + (j >> 2);
#define NEXT_A(s) ((s) == 2 * K1_ASLOT ? 0u : (s) + K1_ASLOT)
#define NEXT_B(s) ((s) == 4 * K1_BSLOT ? 0u : (s) + K1_BSLOT)

    if (wid >= 8) {
        const unsigned short* X = br ? xB : xA;
        const unsigned short* W = br ? wB : wA;
        const __amdgpu_buffer_rsrc_t rX = mk_rsrc(X + (size_t)mb * 256 * DM, 256 * DM * 2);
        const __amdgpu_buffer_rsrc_t rW = mk_rsrc(W, DM * 3 * DM * 2);
        const int hw = wid - 8;
        unsigned voA[2], voB[3];
#pragma unroll
        for (int i = 0; i < 2; ++i) {
            const int R = hw * 64 + i * 8 + (lane >> 3);
            voA[i] = R * (DM * 2) + ((lane & 7) ^ ((R >> 1) & 7)) * 16;
        }
#pragma unroll
        for (int i = 0; i < 3; ++i) {
            const int blk = i * 4 + (lane >> 4), kg = hw * 2 + blk / 6, cb = blk % 6;
            const int hd = k1_head(br, hp, cb >> 1, cb & 1);
            voB[i] = (kg * 4 + ((lane >> 2) & 3)) * (3 * DM * 2) + ((cb >> 1) * DM + hd * 32 + ((lane & 3) ^ ((hw & 1) * 2)) * 8) * 2;
        }
        const unsigned dA = lds0 + hw * 8192, dB = lds0 + K1_BOFF + hw * 3072;
#define K1_DMA_A(t, slot) do { _Pragma("unroll") for (int i_ = 0; i_ < 8; ++i_) \
            dma16(rX, voA[i_ & 1], (unsigned)(t) * 128u + (i_ >> 1) * (16u * DM * 2), dA + (slot) + i_ * 1024); } while (0)
#define K1_DMA_B(u, slot) do { _Pragma("unroll") for (int i_ = 0; i_ < 3; ++i_) \
            dma16(rW, voB[i_], (unsigned)(u) * (32u * 3 * DM * 2), dB + (slot) + i_ * 1024); } while (0)
        K1_DMA_B(0, 0); K1_DMA_A(0, 0);
        K1_DMA_B(1, K1_BSLOT);
        WAIT_BAR(3);
        K1_DMA_B(2, 2 * K1_BSLOT); K1_DMA_A(1, K1_ASLOT);
        K1_DMA_B(3, 3 * K1_BSLOT);
        WAIT_BAR(14);
        K1_DMA_B(4, 4 * K1_BSLOT); K1_DMA_A(2, 2 * K1_ASLOT);
        K1_DMA_B(5, 0);
        WAIT_BAR(17);
        unsigned sa = 0, sb = K1_BSLOT;
#pragma unroll 1
        for (int t = 0; t < 13; ++t) {
            K1_DMA_B(2 * t + 6, sb); K1_DMA_A(t + 3, sa);
            sb = NEXT_B(sb); sa = NEXT_A(sa);
            WAIT_BAR(25);
            K1_DMA_B(2 * t + 7, sb);
            sb = NEXT_B(sb);
            WAIT_BAR(17);
        }
        WAIT_BAR(14);
        WAIT_BAR(3);
        WAIT_BAR(0);
        return;
    }

    if (blockIdx.x == 0 && tid < 32) ((unsigned*)(ws + WS_CNT))[tid * 32] = 0u;
    const int c16 = lane & 15, g = lane >> 4;
    unsigned rdA[2], rdB[2];
#pragma unroll
    for (int u = 0; u < 2; ++u) {
        rdA[u] = (wm * 64 + c16) * 128 + (((4 * u + g) ^ ((c16 >> 1) & 7)) * 16);
        rdB[u] = K1_BOFF + g * 3072 + (c16 >> 2) * 64 + ((u ^ (g & 1)) * 32) + (lane & 3) * 8 + hh * 256;
    }
    f32x4 acc[4][6];
    {
        const float init = br ? 1e-6f : 0.f;
#pragma unroll
        for (int i = 0; i < 4; ++i)
#pragma unroll
            for (int c = 0; c < 6; ++c) { const float v = c < 4 ? init : 0.f; acc[i][c] = f32x4{v, v, v, v}; }
    }
    bf16x8 a0[4], a1[4], bb[6];
#define RDA(A, i, u, sa) A[i] = lds_rd128(lds + (sa) + rdA[u] + (i) * 2048)
#define RDB(c, sb) bb[c] = cat8(lds_tr(lds + (sb) + rdB[(c) & 1] + ((c) >> 1) * 512), lds_tr(lds + (sb) + rdB[(c) & 1] + ((c) >> 1) * 512 + 1536))
#define MM(A, i, c) do { \
        if ((c) < 2 || ((c) < 4 && !BR)) acc[i][c] = MFMA16(bb[c], A[i], acc[i][c]); \
        else acc[i][c] = MFMA16(A[i], bb[c], acc[i][c]); } while (0)
#define MM2(A, i, c) do { MM(A, i, c); MM(A, (i) + 1, c); } while (0)
#define PH_A(A, sb) do { \
        MM2(A, 0, 0); SBAR(); RDB(3, sb); SBAR(); MM2(A, 2, 0); SBAR(); RDB(4, sb); SBAR(); \
        MM2(A, 0, 1); SBAR(); RDB(5, sb); SBAR(); MM2(A, 2, 1); MM2(A, 0, 2); MM2(A, 2, 2); SBAR(); } while (0)
#define PH_B(A, AN, un, san, sbn) do { \
        MM2(A, 0, 3); SBAR(); RDB(0, sbn); RDA(AN, 0, un, san); SBAR(); MM2(A, 2, 3); SBAR(); RDA(AN, 1, un, san); RDA(AN, 2, un, san); SBAR(); \
        MM2(A, 0, 4); SBAR(); RDA(AN, 3, un, san); RDB(1, sbn); SBAR(); MM2(A, 2, 4); SBAR(); RDB(2, sbn); SBAR(); \
        MM2(A, 0, 5); MM2(A, 2, 5); SBAR(); } while (0)
#define PH_B_LAST(A) do { MM2(A, 0, 3); MM2(A, 2, 3); MM2(A, 0, 4); MM2(A, 2, 4); MM2(A, 0, 5); MM2(A, 2, 5); } while (0)
#define LBAR() do { asm volatile("s_waitcnt lgkmcnt(0)\n\ts_barrier" ::: "memory"); SBAR(); } while (0)

    auto mainloop = [&](auto brc) {
        constexpr bool BR = decltype(brc)::value;
        STAMP(1);
        LBAR();
        STAMP(2);
        RDB(0, 0); RDA(a0, 0, 0, 0); RDA(a0, 1, 0, 0); RDA(a0, 2, 0, 0); RDA(a0, 3, 0, 0); RDB(1, 0); RDB(2, 0);
        unsigned sa = 0, sb = 0;
#pragma unroll 1
        for (int t = 0; t < 15; ++t) {
            const unsigned sb1 = NEXT_B(sb), sb2 = NEXT_B(sb1), sa1 = NEXT_A(sa);
            PH_A(a0, sb);
            LBAR();
            PH_B(a0, a1, 1, sa, sb1);
            PH_A(a1, sb1);
            LBAR();
            PH_B(a1, a0, 0, sa1, sb2);
            sa = sa1; sb = sb2;
        }
        {
            PH_A(a0, 0);
            LBAR();
            PH_B(a0, a1, 1, 0, K1_BSLOT);
            PH_A(a1, K1_BSLOT);
            PH_B_LAST(a1);
        }
    int tz = threadIdx.x; asm volatile("" : "+v"(tz));
    const int tid = tz, c16 = tz & 15, g = (tz >> 4) & 3;
    STAMP(3);
    const int tile0 = mb * 8 + wm * 2;
    const unsigned po = ((g >> 1) * 64 + (g & 1) * 32) * 16;
    const __amdgpu_buffer_rsrc_t rWS = mk_rsrc(ws, 26u << 20);
    const int hq = k1_head(br, hp, 0, hh), hk = k1_head(br, hp, 1, hh);
    if constexpr (!BR) {
        const int hv = k1_head(0, hp, 2, hh);
#pragma unroll
        for (int m = 0; m < 2; ++m) {
#pragma unroll
            for (int a = 0; a < 2; ++a) {
                st16<AUX_A>(rWS, WS_KV + (hk * 64 + tile0 + m) * 4096 + po + ((c16 & 7) | (a << 3) | ((c16 >> 3) << 4)) * 16,
                     pack44(acc[2 * m + a][2], acc[2 * m + a][3]));
                st16<AUX_A>(rWS, WS_KV + (hv * 64 + tile0 + m) * 4096 + 2048 + po + (16 * a + c16) * 16, pack44(acc[2 * m][4 + a], acc[2 * m + 1][4 + a]));
                st16<AUX_A>(rWS, WS_Q + (hq * 64 + tile0 + m) * 2048 + po + (16 * a + c16) * 16,
                     pack44(acc[2 * m + a][0] * QSCALE, acc[2 * m + a][1] * QSCALE));
            }
        }
    } else {
        auto phi = [](f32x4& v) {
#pragma unroll
            for (int r = 0; r < 4; r += 2) {
                const f32x2 t = {v[r], v[r + 1]};
                const f32x2 u = t * LOG2E + (-1e-6f * LOG2E);
                const f32x2 e = {__builtin_amdgcn_fmed3f(__builtin_amdgcn_exp2f(u[0]), 0.f, 1.f), __builtin_amdgcn_fmed3f(__builtin_amdgcn_exp2f(u[1]), 0.f, 1.f)};
                const f32x2 mx = {__int_as_float(max(__float_as_int(t[0]), 0x358637bd)), __int_as_float(max(__float_as_int(t[1]), 0x358637bd))};
                const f32x2 o2 = mx + e;
                v[r] = o2[0]; v[r + 1] = o2[1];
            }
        };
        f32x4 kvt[2][2] = {}, zt[2] = {};
        bf16x8 ones;
#pragma unroll
        for (int e = 0; e < 8; ++e) ones[e] = (short)0x3F80;
#pragma unroll
        for (int m = 0; m < 2; ++m) {
            bf16x8 vf[2];
#pragma unroll
            for (int b = 0; b < 2; ++b) vf[b] = __builtin_bit_cast(bf16x8, pack44(acc[2 * m][4 + b], acc[2 * m + 1][4 + b]));
#pragma unroll
            for (int b = 0; b < 2; ++b) {
                phi(acc[2 * m][2 + b]); phi(acc[2 * m + 1][2 + b]);
                const bf16x8 pk = __builtin_bit_cast(bf16x8, pack44(acc[2 * m][2 + b], acc[2 * m + 1][2 + b]));
                kvt[b][0] = MFMA16(pk, vf[0], kvt[b][0]);
                kvt[b][1] = MFMA16(pk, vf[1], kvt[b][1]);
                zt[b] = MFMA16(pk, ones, zt[b]);
            }
        }
#pragma unroll
        for (int i = 0; i < 2; ++i) {
            phi(acc[i][0]); phi(acc[i][1]);
            st16<AUX_B>(rWS, WS_PQ + (hq * 64 + tile0) * 2048 + po + (16 * i + c16) * 16, pack44(acc[i][0], acc[i][1]));
        }
        lds_char* red = (lds_char*)smem + K1_ASLOT;
#pragma unroll
        for (int e = 0; e < 2; ++e) {
            lds_char* rp = red + wid * 4096 + (g >> 1) * 2048 + ((g & 1) * 32 + 16 * e + c16) * 8;
            *LDSP(f32x2, rp) = f32x2{kvt[0][e][0], kvt[0][e][1]};
            *LDSP(f32x2, rp + 512) = f32x2{kvt[0][e][2], kvt[0][e][3]};
            *LDSP(f32x2, rp + 1024) = f32x2{kvt[1][e][0], kvt[1][e][1]};
            *LDSP(f32x2, rp + 1536) = f32x2{kvt[1][e][2], kvt[1][e][3]};
        }
        if (c16 == 0) {
            lds_char* zp = red + 32768 + wid * 128 + (g & 1) * 64 + (g >> 1) * 32;
            *LDSP(f32x4, zp) = zt[0];
            *LDSP(f32x4, zp + 16) = zt[1];
        }
#pragma unroll
        for (int i = 2; i < 4; ++i) {
            phi(acc[i][0]); phi(acc[i][1]);
            st16<AUX_B>(rWS, WS_PQ + (hq * 64 + tile0 + 1) * 2048 + po + (16 * (i - 2) + c16) * 16, pack44(acc[i][0], acc[i][1]));
        }
        asm volatile("s_waitcnt lgkmcnt(0)\n\ts_barrier" ::: "memory");
        {
            const int idx = tid & 255;
            f32x4 s4 = *LDSP(const f32x4, red + (hh * 4 + 0) * 4096 + idx * 16);
#pragma unroll
            for (int m = 1; m < 4; ++m) s4 += *LDSP(const f32x4, red + (hh * 4 + m) * 4096 + idx * 16);
            st16<AUX_B>(rWS, WS_KVP + ((hk * 8 + mb) * 256 + idx) * 16, __builtin_bit_cast(u32x4, s4));
            if (idx < 32) {
                float z = 0.f;
#pragma unroll
                for (int m = 0; m < 4; ++m) z += *LDSP(const float, red + 32768 + (hh * 4 + m) * 128 + idx * 4);
                ((float*)(ws + WS_ZP))[(hk * 8 + mb) * 32 + idx] = z;
            }
        }
    }
    };
    if (br) mainloop(std::integral_constant<bool, true>{}); else mainloop(std::integral_constant<bool, false>{});
    STAMP_FLUSH(0);
#undef RDA
#undef RDB
#undef MM
#undef MM2
#undef PH_A
#undef PH_B
#undef PH_B_LAST
#undef LBAR
#undef K1_DMA_A
#undef K1_DMA_B
}

constexpr int K2_SLOT = 16384, K2_XOFF = 8 * K2_SLOT, K2_LDS = K2_XOFF + 4096 + 1024;
constexpr unsigned K2_BARS = 0x1117;

DI float xhalf_max(float m) {
    auto rr = __builtin_amdgcn_permlane32_swap(__float_as_uint(m), __float_as_uint(m), false, false);
    return __builtin_fmaxf(__uint_as_float(rr[0]), __uint_as_float(rr[1]));
}
DI float xhalf_sum(float m) {
    auto rr = __builtin_amdgcn_permlane32_swap(__float_as_uint(m), __float_as_uint(m), false, false);
    return __uint_as_float(rr[0]) + __uint_as_float(rr[1]);
}
DI float max16(const f32x16& s) {
    float a = __builtin_fmaxf(s[0], s[1]);
#pragma unroll
    for (int r = 2; r < 16; ++r) a = __builtin_fmaxf(a, s[r]);
    return a;
}

constexpr int K3_KG = 1032, K3_WSLOT = 16 * K3_KG, K3_OOFF = 4 * K3_WSLOT, K3_OSLOT = 16384, K23_LDS = K3_OOFF + 5 * K3_OSLOT;
#define NEXT_O(s) ((s) == 4 * K3_OSLOT ? 0u : (s) + K3_OSLOT)

__global__ __launch_bounds__(512, 2) void attn_kernel(char* __restrict__ ws, const unsigned short* __restrict__ woA,
                                                      const unsigned short* __restrict__ woB, unsigned short* __restrict__ out) {
    extern __shared__ __attribute__((aligned(16))) char smem[];
    const lds_char* lds = (const lds_char*)smem;
    const unsigned lds0 = (unsigned)(size_t)lds;
    const int tid = threadIdx.x, lane = tid & 63;
    const int wid = rfl(tid >> 6);
    const int hi = lane >> 5;
    STAMP_DECL; STAMP(0);
    const int xg = blockIdx.x & 7, j0 = blockIdx.x >> 3;
    const int head = xg * 4 + (j0 >> 3), qb = j0 & 7;
    const int br = xg >> 2, rb = (xg & 3) * 4 + (j0 >> 3), cb = j0 & 7;
    unsigned* cnt = (unsigned*)(ws + WS_CNT);
    const __amdgpu_buffer_rsrc_t rWS = mk_rsrc(ws, 26u << 20);
    const __amdgpu_buffer_rsrc_t rKV = mk_rsrc(ws + WS_KV + (size_t)head * 64 * 4096, 64 * 4096);
    const unsigned vl16 = lane * 16;
#define BF(x) __builtin_bit_cast(bf16x8, x)

    if (wid >= 4) {
        const int hw = wid - 4;
        const unsigned vo = hw * 4096 + vl16, dKV = lds0 + hw * 4096;
#define K2_DMA(st) do { const unsigned d_ = dKV + ((st) & 7) * K2_SLOT; \
            dma16(rKV, vo, (unsigned)(st) * K2_SLOT + 4096u, d_); dma16(rKV, vo, (unsigned)(st) * K2_SLOT + 4096u + 1024u, d_ + 1024); \
            dma16(rKV, vo, (unsigned)(st) * K2_SLOT + 4096u + 2048u, d_ + 2048); dma16(rKV, vo, (unsigned)(st) * K2_SLOT + 3072u, d_ + 3072); } while (0)
        K2_DMA(0); K2_DMA(1);
        WAIT_BAR(4);
        K2_DMA(2); K2_DMA(3);
        WAIT_BAR(8);
        K2_DMA(4); K2_DMA(5);
        WAIT_BAR(8);
        K2_DMA(6); K2_DMA(7);
        f32x2 kp[2][8];
        f32x4 zp[8];
        bf16x8 pq[4];
        {
            const unsigned vk = lane * 8, vz = hi * 64;
#pragma unroll
            for (int m = 0; m < 8; ++m) {
                ASM_LD8(kp[0][m], rWS, vk, (unsigned)(WS_KVP + (head * 8 + m) * 4096 + hw * 1024));
                ASM_LD8(kp[1][m], rWS, vk, (unsigned)(WS_KVP + (head * 8 + m) * 4096 + hw * 1024 + 512));
                ASM_LD16(zp[m], rWS, vz, (unsigned)(WS_ZP + (head * 8 + m) * 128 + hw * 16));
            }
#pragma unroll
            for (int i = 0; i < 4; ++i) ASM_LD16(pq[i], rWS, vl16, (unsigned)(WS_PQ + (head * 64 + qb * 8 + hw * 2) * 2048 + i * 1024));
        }
        WAIT_BAR(28);
        K2_DMA(8); K2_DMA(9); K2_DMA(10); K2_DMA(11);
        asm volatile("s_waitcnt vmcnt(16)"
                     : "+v"(kp[0][0]), "+v"(kp[0][1]), "+v"(kp[0][2]), "+v"(kp[0][3]), "+v"(kp[0][4]), "+v"(kp[0][5]), "+v"(kp[0][6]), "+v"(kp[0][7]),
                       "+v"(kp[1][0]), "+v"(kp[1][1]), "+v"(kp[1][2]), "+v"(kp[1][3]), "+v"(kp[1][4]), "+v"(kp[1][5]), "+v"(kp[1][6]), "+v"(kp[1][7]) :: "memory");
        asm volatile("" : "+v"(zp[0]), "+v"(zp[1]), "+v"(zp[2]), "+v"(zp[3]), "+v"(zp[4]), "+v"(zp[5]), "+v"(zp[6]), "+v"(zp[7]),
                          "+v"(pq[0]), "+v"(pq[1]), "+v"(pq[2]), "+v"(pq[3]));
        {
            f32x2 a0 = kp[0][0], a1 = kp[1][0];
            f32x4 z = zp[0];
#pragma unroll
            for (int m = 1; m < 8; ++m) { a0 += kp[0][m]; a1 += kp[1][m]; z += zp[m]; }
            typedef __attribute__((ext_vector_type(2))) unsigned u32x2;
            lds_char* xs = (lds_char*)smem + K2_XOFF + (hw >> 1) * 1024 + vl16 + (hw & 1) * 8;
            *LDSP(u32x2, xs) = u32x2{cvtpk(a0[0], a0[1]), cvtpk(a1[0], a1[1])};
            *LDSP(u32x2, xs + 2048) = u32x2{cvtpk(z[0], z[1]), cvtpk(z[2], z[3])};
        }
        WAIT_BAR(0);
        K2_DMA(12); K2_DMA(13); K2_DMA(14); K2_DMA(15);
        {
            const __amdgpu_buffer_rsrc_t rW = mk_rsrc((const char*)((xg >> 2) ? woB : woA) + j0 * 65536 + hw * 16384, 16384);
#pragma unroll
            for (int i = 0; i < 16; ++i) dma16(rW, vl16, (unsigned)(i * 1024), lds0 + K2_XOFF + 4096);
        }
        {
            const lds_char* xs = lds + K2_XOFF + vl16;
            const bf16x8 kv0 = lds_rd128(xs), kv1 = lds_rd128(xs + 1024), z0 = lds_rd128(xs + 2048), z1 = lds_rd128(xs + 3072);
#pragma unroll
            for (int u = 0; u < 2; ++u) {
                f32x16 num = {}, den = {};
                num = MFMA32(kv0, pq[2 * u], num); den = MFMA32(z0, pq[2 * u], den);
                num = MFMA32(kv1, pq[2 * u + 1], num); den = MFMA32(z1, pq[2 * u + 1], den);
                const float rd = 1.0f / (den[0] + 1e-6f);
                num *= rd;
                const unsigned ob = WS_O + ((64 + qb * 8 + hw * 2 + u) * 32 + head) * 2048 + vl16;
                st16<AUX_OB>(rWS, ob, pack8(num, 0));
                st16<AUX_OB>(rWS, ob + 1024, pack8(num, 1));
            }
        }
        asm volatile("s_waitcnt vmcnt(%0) lgkmcnt(0)\n\ts_barrier" :: "i"((PREFETCH_WO ? 16 : 0) + 4) : "memory");
        asm volatile("s_waitcnt vmcnt(0)" ::: "memory");
        if (lane == 0) __hip_atomic_fetch_add(cnt + (16 + qb * 2 + (hw >> 1)) * 32, 1u, __ATOMIC_RELAXED, __HIP_MEMORY_SCOPE_AGENT);

        const __amdgpu_buffer_rsrc_t rW3 = mk_rsrc(br ? woB : woA, DM * DM * 2);
        const __amdgpu_buffer_rsrc_t rO = mk_rsrc(ws + WS_O + (size_t)(br * 64 + rb * 4 + hw) * 65536, 65536);
        const unsigned voW = (hw * 16 + ((lane >> 2) & 3)) * (DM * 2) + (cb * 128 + (lane >> 4) * 32 + (lane & 3) * 8) * 2;
        const unsigned dW = lds0 + hw * 4 * K3_KG, dO = lds0 + K3_OOFF + hw * 4096;
#define K3_DW(i) do { _Pragma("unroll") for (int n_ = 0; n_ < 4; ++n_) \
            dma16(rW3, voW, (unsigned)(i) * (64u * DM * 2) + n_ * (4u * DM * 2), dW + ((i) & 3) * K3_WSLOT + n_ * K3_KG); } while (0)
#define K3_DO(i, so) do { _Pragma("unroll") for (int n_ = 0; n_ < 4; ++n_) \
            dma16_sc1(rO, vl16, (unsigned)(i) * 4096u + n_ * 1024u, dO + (so) + n_ * 1024); } while (0)
        K3_DW(0); K3_DW(1); K3_DW(2);
        asm volatile("s_barrier" ::: "memory");
        K3_DW(3);
        if (hw == 0) {
            const unsigned* c = cnt + (br * 16 + rb) * 32;
            for (unsigned spin = 0; spin < (1u << 22); ++spin) {
                if (rfl(__hip_atomic_load(c, __ATOMIC_RELAXED, __HIP_MEMORY_SCOPE_AGENT)) >= 64u) break;
                __builtin_amdgcn_s_sleep(POLL_SLEEP);
            }
        }
        asm volatile("s_barrier" ::: "memory");
        K3_DO(0, 0); K3_DO(1, K3_OSLOT); K3_DO(2, 2 * K3_OSLOT); K3_DO(3, 3 * K3_OSLOT); K3_DO(4, 4 * K3_OSLOT);
        WAIT_BAR(16);
        WAIT_BAR(12);
        K3_DW(4); K3_DO(5, 0);
        WAIT_BAR(16);
        unsigned so = K3_OSLOT;
#pragma unroll 1
        for (int i = 2; i < 12; ++i) {
            K3_DW(i + 3); K3_DO(i + 4, so);
            so = NEXT_O(so);
            WAIT_BAR(20);
        }
        K3_DW(15);
        WAIT_BAR(16);
        WAIT_BAR(8);
        WAIT_BAR(0);
        return;
    }

    const int qt0 = qb * 8 + wid * 2;
    bf16x8 qa0, qa1, qb0, qb1, kx0, kx1, ky0, ky1, v0, v1;
    {
        const unsigned sq = WS_Q + (head * 64 + qt0) * 2048;
        ASM_LD16(qa0, rWS, vl16, sq); ASM_LD16(qa1, rWS, vl16, sq + 1024);
        ASM_LD16(kx0, rKV, vl16, 0u); ASM_LD16(kx1, rKV, vl16, 1024u);
        ASM_LD16(qb0, rWS, vl16, sq + 2048); ASM_LD16(qb1, rWS, vl16, sq + 3072);
        ASM_LD16(v0, rKV, vl16, 2048u);
    }
    bf16x8 sel;
    {
        const short v = (((lane >> 4) & 1) == (((lane & 15) >> 2) & 1)) ? (short)0x3F80 : (short)0;
#pragma unroll
        for (int e = 0; e < 8; ++e) sel[e] = v;
    }
    const lds_char* fb = lds + vl16;
    f32x16 sA, sB, nmA, nmB, oA = {}, oB = {};
    f32x4 lA = {}, lB = {};
    u32x4 p0, p1 = {};
    STAMP(1);
    asm volatile("s_waitcnt vmcnt(0)" : "+v"(qa0), "+v"(qa1), "+v"(qb0), "+v"(qb1), "+v"(kx0), "+v"(kx1), "+v"(v0) :: "memory");
    STAMP(2);
    {
        f32x16 z16 = {};
        sA = MFMA32(kx0, qa0, z16); sB = MFMA32(kx0, qb0, z16);
        sA = MFMA32(kx1, qa1, sA); sB = MFMA32(kx1, qb1, sB);
        const float ma = xhalf_max(max16(sA)), mb = xhalf_max(max16(sB));
#pragma unroll
        for (int r = 0; r < 16; ++r) { nmA[r] = -ma; nmB[r] = -mb; sA[r] -= ma; }
    }
    PIN(nmA); PIN(nmB);
    v1 = v0;

#define EX(X, i) X[i] = __builtin_amdgcn_exp2f(X[i])
#define CV(P, n, X, i) P[n] = cvtpk(X[i], X[(i) + 1])
#define ATT(SC, SN, K0, K1, Q0, Q1, NM, OP, LP, OC, LC, R1, R2, R3, R6) do { \
        SN = MFMA32(K0, Q0, NM); \
        SBAR(); R1; EX(SC, 0); EX(SC, 1); EX(SC, 2); CV(p0, 0, SC, 0); SBAR(); \
        OP = MFMA32(v1, BF(p1), OP); \
        SBAR(); R2; EX(SC, 3); EX(SC, 4); CV(p0, 1, SC, 2); EX(SC, 5); SBAR(); \
        SN = MFMA32(K1, Q1, SN); \
        SBAR(); R3; EX(SC, 6); CV(p0, 2, SC, 4); EX(SC, 7); EX(SC, 8); SBAR(); \
        LP = MFMA16(sel, BF(p1), LP); \
        SBAR(); CV(p0, 3, SC, 6); SBAR(); EX(SC, 9); EX(SC, 10); SBAR(); \
        OC = MFMA32(v0, BF(p0), OC); \
        SBAR(); CV(p1, 0, SC, 8); EX(SC, 11); EX(SC, 12); CV(p1, 1, SC, 10); EX(SC, 13); SBAR(); \
        LC = MFMA16(sel, BF(p0), LC); \
        SBAR(); R6; EX(SC, 14); EX(SC, 15); CV(p1, 2, SC, 12); CV(p1, 3, SC, 14); SBAR(); \
        } while (0)
#define TILE2(KC0, KC1, KN0, KN1, TP) do { \
        ATT(sA, sB, KC0, KC1, qb0, qb1, nmB, oB, lB, oA, lA, KN0 = lds_rd128(TP), KN1 = lds_rd128((TP) + 1024), v1 = lds_rd128((TP) + 3072), (void)0); \
        ATT(sB, sA, KN0, KN1, qa0, qa1, nmA, oA, lA, oB, lB, (void)0, (void)0, (void)0, v0 = lds_rd128((TP) + 2048)); } while (0)

    STAMP(3);
#pragma unroll 1
    for (int k = 0; k < 16; ++k) {
        if ((K2_BARS >> k) & 1) asm volatile("s_waitcnt lgkmcnt(0)\n\ts_barrier" ::: "memory");
        SBAR();
        const lds_char* cur = fb + (k & 7) * K2_SLOT;
        TILE2(kx0, kx1, ky0, ky1, cur);
        TILE2(ky0, ky1, kx0, kx1, cur + 4096);
        TILE2(kx0, kx1, ky0, ky1, cur + 2 * 4096);
        TILE2(ky0, ky1, kx0, kx1, cur + 3 * 4096);
    }
    oB = MFMA32(v1, BF(p1), oB);
    lB = MFMA16(sel, BF(p1), lB);
    STAMP(4);
    auto finish = [&](f32x16 o, float l, const bf16x8 q0, const bf16x8 q1, int qt) {
        float t = 0.f;
        bool nan_o = false;
#pragma unroll
        for (int r = 0; r < 16; ++r) { t = __builtin_fmaxf(t, __builtin_fabsf(o[r])); nan_o |= (o[r] != o[r]); }
        const bool bad = !(l < 1.8446744e19f) || !(t < 3.0e38f) || nan_o;
        if (__builtin_expect(__any(bad), 0)) {
            const u32x4* KV = (const u32x4*)(ws + WS_KV) + (size_t)head * 64 * 256 + lane;
            float m = -INFINITY, ls = 0.f;
#pragma unroll
            for (int r = 0; r < 16; ++r) o[r] = 0.f;
#pragma unroll 1
            for (int jt = 0; jt < 64; ++jt) {
                const bf16x8 a0 = BF(KV[jt * 256]), a1 = BF(KV[jt * 256 + 64]), b0 = BF(KV[jt * 256 + 128]), b1 = BF(KV[jt * 256 + 192]);
                f32x16 sc = {};
                sc = MFMA32(a0, q0, sc); sc = MFMA32(a1, q1, sc);
                const float mn = __builtin_fmaxf(m, xhalf_max(max16(sc)));
                const float al = __builtin_amdgcn_exp2f(m - mn);
                m = mn; ls *= al;
#pragma unroll
                for (int r = 0; r < 16; ++r) { o[r] *= al; sc[r] = __builtin_amdgcn_exp2f(sc[r] - mn); ls += sc[r]; }
                o = MFMA32(b0, pack8(sc, 0), o); o = MFMA32(b1, pack8(sc, 1), o);
            }
            l = xhalf_sum(ls);
        }
        o *= 1.0f / l;
        const unsigned ob = WS_O + (qt * 32 + head) * 2048 + vl16;
        st16<AUX_O>(rWS, ob, pack8(o, 0));
        st16<AUX_O>(rWS, ob + 1024, pack8(o, 1));
    };
    finish(oA, lA[0], qa0, qa1, qt0);
    finish(oB, lB[0], qb0, qb1, qt0 + 1);
    asm volatile("s_waitcnt vmcnt(0)" ::: "memory");
    if (lane == 0) __hip_atomic_fetch_add(cnt + (qb * 2 + (wid >> 1)) * 32, 1u, __ATOMIC_RELAXED, __HIP_MEMORY_SCOPE_AGENT);
#undef EX
#undef ATT
#undef CV
#undef TILE2
#undef K2_DMA

    {
    STAMP_DECL; STAMP(0);
    const int wr = wid & 1, wc = wid >> 1, c16 = lane & 15, g = lane >> 4;
    const unsigned rdO = K3_OOFF + wr * 8192 + (g >> 1) * 1024 + ((g & 1) * 32 + c16) * 16;
    const unsigned rdW = ((g >> 1) * 4 + (g & 1)) * K3_KG + wc * 512 + (c16 >> 2) * 64 + (lane & 3) * 16;
    f32x4 acc[4][4] = {};
    bf16x8 o0[4], o1[4];
    s16x4 w0[4][2], w1[4][2];
#define K3_RO(O, i, so, hd) O[i] = lds_rd128(lds + (so) + rdO + ((i) >> 1) * 4096 + ((i) & 1) * 256 + (hd) * 2048)
#define K3_RWH(Wf, ct, t, sw, hd) Wf[ct][t] = lds_tr(lds + (sw) + rdW + (hd) * 8 * K3_KG + (t) * 2 * K3_KG + ((ct) >> 1) * 256 + ((ct) & 1) * 8)
#define K3_RW(Wf, ct, sw, hd) do { K3_RWH(Wf, ct, 0, sw, hd); K3_RWH(Wf, ct, 1, sw, hd); } while (0)
#define K3_MM(OC, WC, i, ct) acc[i][ct] = MFMA16(cat8(WC[ct][0], WC[ct][1]), OC[i], acc[i][ct])
#define K3_MR(OC, WC, i, ct, RD) do { K3_MM(OC, WC, i, ct); SBAR(); RD; SBAR(); } while (0)
#define K3_STEP(OC, WC, ON, WN, so, sw, hd) do { \
        K3_MR(OC, WC, 0, 0, K3_RWH(WN, 0, 0, sw, hd)); K3_MR(OC, WC, 1, 0, K3_RWH(WN, 0, 1, sw, hd)); \
        K3_MR(OC, WC, 2, 0, K3_RO(ON, 0, so, hd));     K3_MR(OC, WC, 3, 0, K3_RO(ON, 1, so, hd)); \
        K3_MR(OC, WC, 0, 1, K3_RO(ON, 2, so, hd));     K3_MR(OC, WC, 1, 1, K3_RO(ON, 3, so, hd)); \
        K3_MR(OC, WC, 2, 1, K3_RWH(WN, 1, 0, sw, hd)); K3_MR(OC, WC, 3, 1, K3_RWH(WN, 1, 1, sw, hd)); \
        K3_MR(OC, WC, 0, 2, K3_RWH(WN, 2, 0, sw, hd)); K3_MR(OC, WC, 1, 2, K3_RWH(WN, 2, 1, sw, hd)); \
        K3_MR(OC, WC, 2, 2, K3_RWH(WN, 3, 0, sw, hd)); K3_MR(OC, WC, 3, 2, K3_RWH(WN, 3, 1, sw, hd)); \
        K3_MM(OC, WC, 0, 3); K3_MM(OC, WC, 1, 3); K3_MM(OC, WC, 2, 3); K3_MM(OC, WC, 3, 3); SBAR(); } while (0)
#define LBAR() do { asm volatile("s_waitcnt lgkmcnt(0)\n\ts_barrier" ::: "memory"); SBAR(); } while (0)
    LBAR();
    LBAR();
    STAMP(1);
    LBAR();
    STAMP(2);
    K3_RW(w0, 0, 0, 0); K3_RO(o0, 0, 0, 0); K3_RO(o0, 1, 0, 0); K3_RO(o0, 2, 0, 0); K3_RO(o0, 3, 0, 0); K3_RW(w0, 1, 0, 0); K3_RW(w0, 2, 0, 0); K3_RW(w0, 3, 0, 0);
    unsigned so = 0;
#pragma unroll 1
    for (int i = 0; i < 15; ++i) {
        const unsigned sw = (i & 3) * K3_WSLOT, swn = ((i + 1) & 3) * K3_WSLOT, son = NEXT_O(so);
        K3_STEP(o0, w0, o1, w1, so, sw, 1);
        LBAR();
        K3_STEP(o1, w1, o0, w0, son, swn, 0);
        so = son;
    }
    K3_STEP(o0, w0, o1, w1, 0, 3 * K3_WSLOT, 1);
#pragma unroll
    for (int i = 0; i < 4; ++i)
#pragma unroll
        for (int ct = 0; ct < 4; ++ct) K3_MM(o1, w1, i, ct);
    STAMP(3);
    {
        const __amdgpu_buffer_rsrc_t rOut = mk_rsrc(out, SEQ * 2 * DM * 2);
        const unsigned ob = ((rb * 128 + wr * 64 + c16) * (2 * DM) + br * DM + cb * 128 + wc * 64 + g * 8) * 2;
#pragma unroll
        for (int i = 0; i < 4; ++i)
#pragma unroll
            for (int u = 0; u < 2; ++u)
                st16<AUX_OUT>(rOut, ob + i * 16 * (2 * DM * 2) + u * 64, pack44(acc[i][2 * u], acc[i][2 * u + 1]));
    }
    STAMP_FLUSH(2);
    }
#undef K3_DW
#undef K3_DO
#undef K3_RO
#undef K3_RW
#undef K3_RWH
#undef K3_MR
#undef K3_MM
#undef K3_STEP
#undef LBAR
}

extern "C" void kernel_launch(void* const* d_in, const int* in_sizes, int n_in, void* d_out, int out_size, void* d_ws,
                              size_t ws_size, hipStream_t stream) {
    const unsigned short* xA = (const unsigned short*)d_in[0];
    const unsigned short* wqkvA = (const unsigned short*)d_in[1];
    const unsigned short* woA = (const unsigned short*)d_in[2];
    const unsigned short* xB = (const unsigned short*)d_in[3];
    const unsigned short* wqkvB = (const unsigned short*)d_in[4];
    const unsigned short* woB = (const unsigned short*)d_in[5];
    char* ws = (char*)d_ws;
    (void)hipFuncSetAttribute((const void*)qkv_kernel, hipFuncAttributeMaxDynamicSharedMemorySize, K1_LDS);
    (void)hipFuncSetAttribute((const void*)attn_kernel, hipFuncAttributeMaxDynamicSharedMemorySize, K23_LDS);
    qkv_kernel<<<256, 768, K1_LDS, stream>>>(xA, wqkvA, xB, wqkvB, ws);
    attn_kernel<<<256, 512, K23_LDS, stream>>>(ws, woA, woB, (unsigned short*)d_out);
}
```

```cpp
#include <hip/hip_runtime.h>
#include <hip/hip_bf16.h>

typedef __attribute__((ext_vector_type(8))) short bf16x8;
typedef __attribute__((ext_vector_type(4))) short s16x4;
typedef __attribute__((ext_vector_type(16))) float f32x16;
typedef __attribute__((ext_vector_type(4))) float f32x4;
typedef __attribute__((ext_vector_type(2))) float f32x2;
typedef __attribute__((ext_vector_type(4))) unsigned u32x4;
typedef __attribute__((address_space(3))) char lds_char;
#define LDSP(T, p) ((__attribute__((address_space(3))) T*)(p))

#define DI __device__ __forceinline__
#define MFMA32(a, b, c) __builtin_amdgcn_mfma_f32_32x32x16_bf16((a), (b), (c), 0, 0, 0)
#define MFMA16(a, b, c) __builtin_amdgcn_mfma_f32_16x16x32_bf16((a), (b), (c), 0, 0, 0)
#define SBAR() __builtin_amdgcn_sched_barrier(0)
#define PIN(x) asm volatile("" : "+v"(x))
#define WAIT_BAR(N) asm volatile("s_waitcnt vmcnt(" #N ") lgkmcnt(0)\n\ts_barrier" ::: "memory")

#define STAMP_DECL
#define STAMP(i) do {} while (0)
#define STAMP_FLUSH(kid) do {} while (0)
constexpr int SEQ = 2048, DM = 1024, NH = 32;
constexpr float LOG2E = 1.4426950408889634f;
constexpr float QSCALE = 0.17677669529663687f * LOG2E;

constexpr size_t WS_Q = 0;
constexpr size_t WS_KV = (size_t)4 << 20;
constexpr size_t WS_PQ = (size_t)12 << 20;
constexpr size_t WS_O = (size_t)16 << 20;
constexpr size_t WS_KVP = (size_t)24 << 20;
constexpr size_t WS_ZP = (size_t)25 << 20;
constexpr size_t WS_CNT = WS_ZP + 65536;

typedef __attribute__((ext_vector_type(2))) __bf16 bf16x2_t;
DI unsigned cvtpk(float lo, float hi) {
    return __builtin_bit_cast(unsigned, __builtin_convertvector((f32x2{lo, hi}), bf16x2_t));
}
DI bf16x8 pack8(const f32x16& x, int s) {
    u32x4 p;
    p[0] = cvtpk(x[8 * s + 0], x[8 * s + 1]);
    p[1] = cvtpk(x[8 * s + 2], x[8 * s + 3]);
    p[2] = cvtpk(x[8 * s + 4], x[8 * s + 5]);
    p[3] = cvtpk(x[8 * s + 6], x[8 * s + 7]);
    return __builtin_bit_cast(bf16x8, p);
}
DI void dma16(__amdgpu_buffer_rsrc_t rsrc, unsigned voff, unsigned soff, unsigned lds_dst) {
    unsigned keep;
    asm volatile("s_mov_b32 %0, m0\n\ts_mov_b32 m0, %4\n\ts_nop 0\n\tbuffer_load_dwordx4 %1, %2, %3 offen lds\n\ts_mov_b32 m0, %0"
                 : "=&s"(keep) : "v"(voff), "s"(rsrc), "s"(soff), "s"(lds_dst) : "memory");
}
DI void dma16_nt(__amdgpu_buffer_rsrc_t rsrc, unsigned voff, unsigned soff, unsigned lds_dst) {
    unsigned keep;
    asm volatile("s_mov_b32 %0, m0\n\ts_mov_b32 m0, %4\n\ts_nop 0\n\tbuffer_load_dwordx4 %1, %2, %3 offen nt lds\n\ts_mov_b32 m0, %0"
                 : "=&s"(keep) : "v"(voff), "s"(rsrc), "s"(soff), "s"(lds_dst) : "memory");
}
#define DMA_X dma16
#define DMA_W dma16_nt
DI void dma16_sc1(__amdgpu_buffer_rsrc_t rsrc, unsigned voff, unsigned soff, unsigned lds_dst) {
    unsigned keep;
    asm volatile("s_mov_b32 %0, m0\n\ts_mov_b32 m0, %4\n\ts_nop 0\n\tbuffer_load_dwordx4 %1, %2, %3 offen sc1 lds\n\ts_mov_b32 m0, %0"
                 : "=&s"(keep) : "v"(voff), "s"(rsrc), "s"(soff), "s"(lds_dst) : "memory");
}
DI void dma16_lo(__amdgpu_buffer_rsrc_t rsrc, unsigned voff, unsigned soff, unsigned lds_dst) {
    unsigned keep;
    asm volatile("s_mov_b32 %0, m0\n\ts_mov_b32 m0, %4\n\ts_mov_b32 exec_hi, 0\n\tbuffer_load_dwordx4 %1, %2, %3 offen lds\n\ts_mov_b32 exec_hi, -1\n\ts_mov_b32 m0, %0"
                 : "=&s"(keep) : "v"(voff), "s"(rsrc), "s"(soff), "s"(lds_dst) : "memory");
}
DI __amdgpu_buffer_rsrc_t mk_rsrc(const void* p, unsigned bytes) {
    return __builtin_amdgcn_make_buffer_rsrc((void*)p, (short)0, (int)bytes, 0x00020000);
}
#define PREFETCH_WO 1
#define K3_ROT 0
#define POLL_SLEEP 6
#define AUX_OUT 16
#define AUX_A 16
#define AUX_B 16
#define AUX_O 16
#define AUX_OB 16
template <int AUX> DI void st16(__amdgpu_buffer_rsrc_t rsrc, unsigned voff, u32x4 v) { __builtin_amdgcn_raw_buffer_store_b128(v, rsrc, voff, 0, AUX); }
template <int AUX> DI void st16(__amdgpu_buffer_rsrc_t rsrc, unsigned voff, bf16x8 v) { st16<AUX>(rsrc, voff, __builtin_bit_cast(u32x4, v)); }
#define ASM_LD16(dst, rsrc, voff, soff) asm volatile("buffer_load_dwordx4 %0, %1, %2, %3 offen" : "=v"(dst) : "v"(voff), "s"(rsrc), "s"(soff) : "memory")
#define ASM_LD8(dst, rsrc, voff, soff) asm volatile("buffer_load_dwordx2 %0, %1, %2, %3 offen" : "=v"(dst) : "v"(voff), "s"(rsrc), "s"(soff) : "memory")
DI bf16x8 lds_rd128(const lds_char* p) { return *LDSP(const bf16x8, p); }
DI s16x4 lds_tr(const lds_char* p) {
    typedef short v4i16_t __attribute__((ext_vector_type(4)));
    return __builtin_bit_cast(s16x4, __builtin_amdgcn_ds_read_tr16_b64_v4i16(LDSP(v4i16_t, p)));
}
DI bf16x8 cat8(s16x4 lo, s16x4 hi) { return __builtin_shufflevector(lo, hi, 0, 1, 2, 3, 4, 5, 6, 7); }
DI unsigned rfl(unsigned x) { return __builtin_amdgcn_readfirstlane(x); }

__device__ __noinline__ __attribute__((used)) void code_mark_a() { asm volatile(""); }
__device__ __noinline__ __attribute__((used)) void code_mark_b();
__device__ __noinline__ __attribute__((used)) void code_mark_c();
#define CODE_PF 3
DI void code_touch(const void* beg, const void* end, int lane, unsigned lds_sink) {
    const __amdgpu_buffer_rsrc_t r = mk_rsrc(beg, (unsigned)((const char*)end - (const char*)beg));
    const unsigned vo = lane * 128;
    unsigned keep;
    asm volatile("s_mov_b32 %0, m0\n\ts_mov_b32 m0, %4\n\ts_nop 4\n\tbuffer_load_dword %1, %2, 0 offen lds\n\tbuffer_load_dword %1, %2, %3 offen lds\n\ts_mov_b32 m0, %0"
                 : "=&s"(keep) : "v"(vo), "s"(r), "s"(8192u), "s"(lds_sink) : "memory");
}

DI u32x4 pack44(const f32x4& a, const f32x4& b) {
    u32x4 p;
    p[0] = cvtpk(a[0], a[1]); p[1] = cvtpk(a[2], a[3]); p[2] = cvtpk(b[0], b[1]); p[3] = cvtpk(b[2], b[3]);
    return p;
}

constexpr int K2_SLOT = 16384, K2_XOFF = 8 * K2_SLOT, K2_LDS = K2_XOFF + 4096 + 1024;
constexpr unsigned K2_BARS = 0x1117;

DI float xhalf_max(float m) {
    auto rr = __builtin_amdgcn_permlane32_swap(__float_as_uint(m), __float_as_uint(m), false, false);
    return __builtin_fmaxf(__uint_as_float(rr[0]), __uint_as_float(rr[1]));
}
DI float xhalf_sum(float m) {
    auto rr = __builtin_amdgcn_permlane32_swap(__float_as_uint(m), __float_as_uint(m), false, false);
    return __uint_as_float(rr[0]) + __uint_as_float(rr[1]);
}
DI float max16(const f32x16& s) {
    float a = __builtin_fmaxf(s[0], s[1]);
#pragma unroll
    for (int r = 2; r < 16; ++r) a = __builtin_fmaxf(a, s[r]);
    return a;
}

constexpr int K3_KG = 1032, K3_WSLOT = 16 * K3_KG, K3_OOFF = 4 * K3_WSLOT, K3_OSLOT = 16384, K23_LDS = K3_OOFF + 5 * K3_OSLOT;
#define NEXT_O(s) ((s) == 4 * K3_OSLOT ? 0u : (s) + K3_OSLOT)

__global__ __launch_bounds__(512, 2) void attn_kernel(char* __restrict__ ws, const unsigned short* __restrict__ woA,
                                                      const unsigned short* __restrict__ woB, unsigned short* __restrict__ out) {
    extern __shared__ __attribute__((aligned(16))) char smem[];
    const lds_char* lds = (const lds_char*)smem;
    const unsigned lds0 = (unsigned)(size_t)lds;
    const int tid = threadIdx.x, lane = tid & 63;
    const int wid = rfl(tid >> 6);
    const int hi = lane >> 5;
    STAMP_DECL; STAMP(0);
    const int xg = blockIdx.x & 7, j0 = blockIdx.x >> 3;
    const int head = xg * 4 + (j0 >> 3), qb = j0 & 7;
    const int br = xg >> 2, rb = (xg & 3) * 4 + (j0 >> 3), cb = j0 & 7;
    unsigned* cnt = (unsigned*)(ws + WS_CNT);
    const __amdgpu_buffer_rsrc_t rWS = mk_rsrc(ws, 26u << 20);
    const __amdgpu_buffer_rsrc_t rKV = mk_rsrc(ws + WS_KV + (size_t)head * 64 * 4096, 64 * 4096);
    const unsigned vl16 = lane * 16;
#define BF(x) __builtin_bit_cast(bf16x8, x)

    if (wid >= 4) {
        const int hw = wid - 4;
        const unsigned vo = hw * 4096 + vl16, dKV = lds0 + hw * 4096;
#define K2_DMA(st) do { const unsigned d_ = dKV + ((st) & 7) * K2_SLOT; \
            dma16(rKV, vo, (unsigned)(st) * K2_SLOT + 4096u, d_); dma16(rKV, vo, (unsigned)(st) * K2_SLOT + 4096u + 1024u, d_ + 1024); \
            dma16(rKV, vo, (unsigned)(st) * K2_SLOT + 4096u + 2048u, d_ + 2048); dma16(rKV, vo, (unsigned)(st) * K2_SLOT + 3072u, d_ + 3072); } while (0)
        K2_DMA(0); K2_DMA(1);
        WAIT_BAR(4);
        K2_DMA(2); K2_DMA(3);
        WAIT_BAR(8);
        K2_DMA(4); K2_DMA(5);
        WAIT_BAR(8);
        K2_DMA(6); K2_DMA(7);
        f32x2 kp[2][8];
        f32x4 zp[8];
        bf16x8 pq[4];
        {
            const unsigned vk = lane * 8, vz = hi * 64;
#pragma unroll
            for (int m = 0; m < 8; ++m) {
                ASM_LD8(kp[0][m], rWS, vk, (unsigned)(WS_KVP + (head * 8 + m) * 4096 + hw * 1024));
                ASM_LD8(kp[1][m], rWS, vk, (unsigned)(WS_KVP + (head * 8 + m) * 4096 + hw * 1024 + 512));
                ASM_LD16(zp[m], rWS, vz, (unsigned)(WS_ZP + (head * 8 + m) * 128 + hw * 16));
            }
#pragma unroll
            for (int i = 0; i < 4; ++i) ASM_LD16(pq[i], rWS, vl16, (unsigned)(WS_PQ + (head * 64 + qb * 8 + hw * 2) * 2048 + i * 1024));
        }
        WAIT_BAR(28);
        K2_DMA(8); K2_DMA(9); K2_DMA(10); K2_DMA(11);
        asm volatile("s_waitcnt vmcnt(16)"
                     : "+v"(kp[0][0]), "+v"(kp[0][1]), "+v"(kp[0][2]), "+v"(kp[0][3]), "+v"(kp[0][4]), "+v"(kp[0][5]), "+v"(kp[0][6]), "+v"(kp[0][7]),
                       "+v"(kp[1][0]), "+v"(kp[1][1]), "+v"(kp[1][2]), "+v"(kp[1][3]), "+v"(kp[1][4]), "+v"(kp[1][5]), "+v"(kp[1][6]), "+v"(kp[1][7]) :: "memory");
        asm volatile("" : "+v"(zp[0]), "+v"(zp[1]), "+v"(zp[2]), "+v"(zp[3]), "+v"(zp[4]), "+v"(zp[5]), "+v"(zp[6]), "+v"(zp[7]),
                          "+v"(pq[0]), "+v"(pq[1]), "+v"(pq[2]), "+v"(pq[3]));
        {
            f32x2 a0 = kp[0][0], a1 = kp[1][0];
            f32x4 z = zp[0];
#pragma unroll
            for (int m = 1; m < 8; ++m) { a0 += kp[0][m]; a1 += kp[1][m]; z += zp[m]; }
            typedef __attribute__((ext_vector_type(2))) unsigned u32x2;
            lds_char* xs = (lds_char*)smem + K2_XOFF + (hw >> 1) * 1024 + vl16 + (hw & 1) * 8;
            *LDSP(u32x2, xs) = u32x2{cvtpk(a0[0], a0[1]), cvtpk(a1[0], a1[1])};
            *LDSP(u32x2, xs + 2048) = u32x2{cvtpk(z[0], z[1]), cvtpk(z[2], z[3])};
        }
        WAIT_BAR(0);
        K2_DMA(12); K2_DMA(13); K2_DMA(14); K2_DMA(15);
        {
            const __amdgpu_buffer_rsrc_t rW = mk_rsrc((const char*)((xg >> 2) ? woB : woA) + j0 * 65536 + hw * 16384, 16384);
#pragma unroll
            for (int i = 0; i < 16; ++i) dma16(rW, vl16, (unsigned)(i * 1024), lds0 + K2_XOFF + 4096);
        }
        {
            const lds_char* xs = lds + K2_XOFF + vl16;
            const bf16x8 kv0 = lds_rd128(xs), kv1 = lds_rd128(xs + 1024), z0 = lds_rd128(xs + 2048), z1 = lds_rd128(xs + 3072);
#pragma unroll
            for (int u = 0; u < 2; ++u) {
                f32x16 num = {}, den = {};
                num = MFMA32(kv0, pq[2 * u], num); den = MFMA32(z0, pq[2 * u], den);
                num = MFMA32(kv1, pq[2 * u + 1], num); den = MFMA32(z1, pq[2 * u + 1], den);
                const float rd = 1.0f / (den[0] + 1e-6f);
                num *= rd;
                const unsigned ob = WS_O + ((64 + qb * 8 + hw * 2 + u) * 32 + head) * 2048 + vl16;
                st16<AUX_OB>(rWS, ob, pack8(num, 0));
                st16<AUX_OB>(rWS, ob + 1024, pack8(num, 1));
            }
        }
        asm volatile("s_waitcnt vmcnt(%0) lgkmcnt(0)\n\ts_barrier" :: "i"((PREFETCH_WO ? 16 : 0) + 4) : "memory");
        asm volatile("s_waitcnt vmcnt(0)" ::: "memory");
        if (lane == 0) __hip_atomic_fetch_add(cnt + (16 + qb * 2 + (hw >> 1)) * 32, 1u, __ATOMIC_RELAXED, __HIP_MEMORY_SCOPE_AGENT);

        const __amdgpu_buffer_rsrc_t rW3 = mk_rsrc(br ? woB : woA, DM * DM * 2);
        const __amdgpu_buffer_rsrc_t rO = mk_rsrc(ws + WS_O + (size_t)(br * 64 + rb * 4 + hw) * 65536, 65536);
        const unsigned voW = (hw * 16 + ((lane >> 2) & 3)) * (DM * 2) + (cb * 128 + (lane >> 4) * 32 + (lane & 3) * 8) * 2;
        const unsigned dW = lds0 + hw * 4 * K3_KG, dO = lds0 + K3_OOFF + hw * 4096;
#define K3_DW(i) do { _Pragma("unroll") for (int n_ = 0; n_ < 4; ++n_) \
            dma16(rW3, voW, (unsigned)(i) * (64u * DM * 2) + n_ * (4u * DM * 2), dW + ((i) & 3) * K3_WSLOT + n_ * K3_KG); } while (0)
#define K3_DO(i, so) do { _Pragma("unroll") for (int n_ = 0; n_ < 4; ++n_) \
            dma16_sc1(rO, vl16, (unsigned)(i) * 4096u + n_ * 1024u, dO + (so) + n_ * 1024); } while (0)
        K3_DW(0); K3_DW(1); K3_DW(2);
        asm volatile("s_barrier" ::: "memory");
        K3_DW(3);
        if (hw == 0) {
            const unsigned* c = cnt + (br * 16 + rb) * 32;
            for (unsigned spin = 0; spin < (1u << 22); ++spin) {
                if (rfl(__hip_atomic_load(c, __ATOMIC_RELAXED, __HIP_MEMORY_SCOPE_AGENT)) >= 64u) break;
                __builtin_amdgcn_s_sleep(POLL_SLEEP);
            }
        }
        asm volatile("s_barrier" ::: "memory");
        K3_DO(0, 0); K3_DO(1, K3_OSLOT); K3_DO(2, 2 * K3_OSLOT); K3_DO(3, 3 * K3_OSLOT); K3_DO(4, 4 * K3_OSLOT);
        WAIT_BAR(16);
        WAIT_BAR(12);
        K3_DW(4); K3_DO(5, 0);
        WAIT_BAR(16);
        unsigned so = K3_OSLOT;
#pragma unroll 1
        for (int i = 2; i < 12; ++i) {
            K3_DW(i + 3); K3_DO(i + 4, so);
            so = NEXT_O(so);
            WAIT_BAR(20);
        }
        K3_DW(15);
        WAIT_BAR(16);
        WAIT_BAR(8);
        WAIT_BAR(0);
        return;
    }

    const int qt0 = qb * 8 + wid * 2;
    bf16x8 qa0, qa1, qb0, qb1, kx0, kx1, ky0, ky1, v0, v1;
    {
        const unsigned sq = WS_Q + (head * 64 + qt0) * 2048;
        ASM_LD16(qa0, rWS, vl16, sq); ASM_LD16(qa1, rWS, vl16, sq + 1024);
        ASM_LD16(kx0, rKV, vl16, 0u); ASM_LD16(kx1, rKV, vl16, 1024u);
        ASM_LD16(qb0, rWS, vl16, sq + 2048); ASM_LD16(qb1, rWS, vl16, sq + 3072);
        ASM_LD16(v0, rKV, vl16, 2048u);
    }
    bf16x8 sel;
    {
        const short v = (((lane >> 4) & 1) == (((lane & 15) >> 2) & 1)) ? (short)0x3F80 : (short)0;
#pragma unroll
        for (int e = 0; e < 8; ++e) sel[e] = v;
    }
    const lds_char* fb = lds + vl16;
    f32x16 sA, sB, nmA, nmB, oA = {}, oB = {};
    f32x4 lA = {}, lB = {};
    u32x4 p0, p1 = {};
    STAMP(1);
    asm volatile("s_waitcnt vmcnt(0)" : "+v"(qa0), "+v"(qa1), "+v"(qb0), "+v"(qb1), "+v"(kx0), "+v"(kx1), "+v"(v0) :: "memory");
    STAMP(2);
    {
        f32x16 z16 = {};
        sA = MFMA32(kx0, qa0, z16); sB = MFMA32(kx0, qb0, z16);
        sA = MFMA32(kx1, qa1, sA); sB = MFMA32(kx1, qb1, sB);
        const float ma = xhalf_max(max16(sA)), mb = xhalf_max(max16(sB));
#pragma unroll
        for (int r = 0; r < 16; ++r) { nmA[r] = -ma; nmB[r] = -mb; sA[r] -= ma; }
    }
    PIN(nmA); PIN(nmB);
    v1 = v0;

#define EX(X, i) X[i] = __builtin_amdgcn_exp2f(X[i])
#define CV(P, n, X, i) P[n] = cvtpk(X[i], X[(i) + 1])
#define X1(op) do { op; SBAR(); } while (0)
#define ATT(SC, SN, K0, K1, Q0, Q1, NM, VP, OP, LP, VC, OC, LC, R1, R2, R3) do { \
        X1(SN = MFMA32(K0, Q0, NM)); X1(R1); \
        X1(EX(SC, 0)); X1(EX(SC, 1)); X1(p1[3] = cvtpk(e14, e15)); X1(EX(SC, 2)); \
        X1(SN = MFMA32(K1, Q1, SN)); X1(R2); \
        X1(EX(SC, 3)); X1(CV(p0, 0, SC, 0)); X1(EX(SC, 4)); X1(EX(SC, 5)); \
        X1(OP = MFMA32(VP, BF(p1), OP)); X1(R3); \
        X1(CV(p0, 1, SC, 2)); X1(EX(SC, 6)); X1(EX(SC, 7)); X1(EX(SC, 8)); \
        X1(LP = MFMA16(sel, BF(p1), LP)); \
        X1(CV(p0, 2, SC, 4)); X1(EX(SC, 9)); X1(CV(p0, 3, SC, 6)); X1(EX(SC, 10)); X1(EX(SC, 11)); \
        X1(OC = MFMA32(VC, BF(p0), OC)); \
        X1(CV(p1, 0, SC, 8)); X1(EX(SC, 12)); X1(EX(SC, 13)); X1(e14 = __builtin_amdgcn_exp2f(SC[14])); \
        X1(LC = MFMA16(sel, BF(p0), LC)); \
        X1(CV(p1, 1, SC, 10)); X1(e15 = __builtin_amdgcn_exp2f(SC[15])); X1(CV(p1, 2, SC, 12)); \
        } while (0)
#define TILE2(KC0, KC1, V0C, V1P, KN0, KN1, V0N, V1C, TP) do { \
        ATT(sA, sB, KC0, KC1, qb0, qb1, nmB, V1P, oB, lB, V0C, oA, lA, \
            (V0N = lds_rd128((TP) + 2048), V1C = lds_rd128((TP) + 3072)), KN1 = lds_rd128((TP) + 1024), KN0 = lds_rd128(TP)); \
        ATT(sB, sA, KN0, KN1, qa0, qa1, nmA, V1C, oA, lA, V0C, oB, lB, (void)0, (void)0, (void)0); } while (0)
    bf16x8 vy0, vy1;
    float e14 = 0.f, e15 = 0.f;

    STAMP(3);
#pragma unroll 1
    for (int k = 0; k < 16; ++k) {
        if ((K2_BARS >> k) & 1) asm volatile("s_waitcnt lgkmcnt(0)\n\ts_barrier" ::: "memory");
        SBAR();
        const lds_char* cur = fb + (k & 7) * K2_SLOT;
        TILE2(kx0, kx1, v0, v1, ky0, ky1, vy0, vy1, cur);
        TILE2(ky0, ky1, vy0, vy1, kx0, kx1, v0, v1, cur + 4096);
        TILE2(kx0, kx1, v0, v1, ky0, ky1, vy0, vy1, cur + 2 * 4096);
        TILE2(ky0, ky1, vy0, vy1, kx0, kx1, v0, v1, cur + 3 * 4096);
    }
    p1[3] = cvtpk(e14, e15);
    oB = MFMA32(v1, BF(p1), oB);
    lB = MFMA16(sel, BF(p1), lB);
    STAMP(4);
    auto finish = [&](f32x16 o, float l, const bf16x8 q0, const bf16x8 q1, int qt) {
        float t = 0.f;
        bool nan_o = false;
#pragma unroll
        for (int r = 0; r < 16; ++r) { t = __builtin_fmaxf(t, __builtin_fabsf(o[r])); nan_o |= (o[r] != o[r]); }
        const bool bad = !(l < 1.8446744e19f) || !(t < 3.0e38f) || nan_o;
        if (__builtin_expect(__any(bad), 0)) {
            const u32x4* KV = (const u32x4*)(ws + WS_KV) + (size_t)head * 64 * 256 + lane;
            float m = -INFINITY, ls = 0.f;
#pragma unroll
            for (int r = 0; r < 16; ++r) o[r] = 0.f;
#pragma unroll 1
            for (int jt = 0; jt < 64; ++jt) {
                const bf16x8 a0 = BF(KV[jt * 256]), a1 = BF(KV[jt * 256 + 64]), b0 = BF(KV[jt * 256 + 128]), b1 = BF(KV[jt * 256 + 192]);
                f32x16 sc = {};
                sc = MFMA32(a0, q0, sc); sc = MFMA32(a1, q1, sc);
                const float mn = __builtin_fmaxf(m, xhalf_max(max16(sc)));
                const float al = __builtin_amdgcn_exp2f(m - mn);
                m = mn; ls *= al;
#pragma unroll
                for (int r = 0; r < 16; ++r) { o[r] *= al; sc[r] = __builtin_amdgcn_exp2f(sc[r] - mn); ls += sc[r]; }
                o = MFMA32(b0, pack8(sc, 0), o); o = MFMA32(b1, pack8(sc, 1), o);
            }
            l = xhalf_sum(ls);
        }
        o *= 1.0f / l;
        const unsigned ob = WS_O + (qt * 32 + head) * 2048 + vl16;
        st16<AUX_O>(rWS, ob, pack8(o, 0));
        st16<AUX_O>(rWS, ob + 1024, pack8(o, 1));
    };
    finish(oA, lA[0], qa0, qa1, qt0);
    finish(oB, lB[0], qb0, qb1, qt0 + 1);
    asm volatile("s_waitcnt vmcnt(0)" ::: "memory");
    if (lane == 0) __hip_atomic_fetch_add(cnt + (qb * 2 + (wid >> 1)) * 32, 1u, __ATOMIC_RELAXED, __HIP_MEMORY_SCOPE_AGENT);
#undef EX
#undef ATT
#undef CV
#undef TILE2
#undef X1
#undef K2_DMA

    {
    STAMP_DECL; STAMP(0);
    const int wr = wid & 1, wc = wid >> 1, c16 = lane & 15, g = lane >> 4;
    const unsigned rdO = K3_OOFF + wr * 8192 + (g >> 1) * 1024 + ((g & 1) * 32 + c16) * 16;
    const unsigned rdW = ((g >> 1) * 4 + (g & 1)) * K3_KG + wc * 512 + (c16 >> 2) * 64 + (lane & 3) * 16;
    f32x4 acc[4][4] = {};
    bf16x8 o0[4], o1[4];
    s16x4 w0[4][2], w1[4][2];
#define K3_RO(O, i, pO, hd) O[i] = lds_rd128((pO) + ((i) >> 1) * 4096 + ((i) & 1) * 256 + (hd) * 2048)
#define K3_RWH(Wf, ct, t, pW, hd) Wf[ct][t] = lds_tr((pW) + (hd) * 8 * K3_KG + (t) * 2 * K3_KG + ((ct) >> 1) * 256 + ((ct) & 1) * 8)
#define K3_RW(Wf, ct, pW, hd) do { K3_RWH(Wf, ct, 0, pW, hd); K3_RWH(Wf, ct, 1, pW, hd); } while (0)
#define K3_MM(OC, WC, n) acc[(n) & 3][(n) >> 2] = MFMA16(cat8(WC[(n) >> 2][0], WC[(n) >> 2][1]), OC[(n) & 3], acc[(n) & 3][(n) >> 2])
#define K3_MR(OC, WC, n, RD) do { K3_MM(OC, WC, n); SBAR(); RD; SBAR(); } while (0)
#define K3_RUN12(OC, WC, n0, ON, WN, pO, pW, hd) do { \
        K3_MR(OC, WC, (n0) + 0, K3_RWH(WN, 0, 0, pW, hd)); K3_MR(OC, WC, (n0) + 1, K3_RWH(WN, 0, 1, pW, hd)); \
        K3_MR(OC, WC, (n0) + 2, K3_RO(ON, 0, pO, hd));     K3_MR(OC, WC, (n0) + 3, K3_RO(ON, 1, pO, hd)); \
        K3_MR(OC, WC, (n0) + 4, K3_RO(ON, 2, pO, hd));     K3_MR(OC, WC, (n0) + 5, K3_RO(ON, 3, pO, hd)); \
        K3_MR(OC, WC, (n0) + 6, K3_RWH(WN, 1, 0, pW, hd)); K3_MR(OC, WC, (n0) + 7, K3_RWH(WN, 1, 1, pW, hd)); \
        K3_MR(OC, WC, (n0) + 8, K3_RWH(WN, 2, 0, pW, hd)); K3_MR(OC, WC, (n0) + 9, K3_RWH(WN, 2, 1, pW, hd)); \
        K3_MR(OC, WC, (n0) + 10, K3_RWH(WN, 3, 0, pW, hd)); K3_MR(OC, WC, (n0) + 11, K3_RWH(WN, 3, 1, pW, hd)); } while (0)
#define LBAR() do { asm volatile("s_waitcnt lgkmcnt(0)\n\ts_barrier" ::: "memory"); SBAR(); } while (0)
    LBAR();
    LBAR();
    STAMP(1);
    LBAR();
    STAMP(2);
    const lds_char* pO = lds + rdO;
    const lds_char* pW = lds + rdW;
    K3_RW(w0, 0, pW, 0); K3_RO(o0, 0, pO, 0); K3_RO(o0, 1, pO, 0); K3_RO(o0, 2, pO, 0); K3_RO(o0, 3, pO, 0); K3_RW(w0, 1, pW, 0); K3_RW(w0, 2, pW, 0); K3_RW(w0, 3, pW, 0);
    __builtin_amdgcn_s_waitcnt(0xC07F);
    SBAR();
    unsigned so = 0, sw = 0;
#pragma unroll 1
    for (int i = 0; i < 15; ++i) {
        K3_RUN12(o0, w0, 0, o1, w1, pO, pW, 1);
        K3_MR(o0, w0, 12, so = NEXT_O(so));
        K3_MR(o0, w0, 13, sw = (sw == 3 * K3_WSLOT ? 0u : sw + K3_WSLOT));
        K3_MR(o0, w0, 14, pO = lds + rdO + so; PIN(pO));
        K3_MR(o0, w0, 15, pW = lds + rdW + sw; PIN(pW));
        K3_MM(o1, w1, 0); K3_MM(o1, w1, 1); K3_MM(o1, w1, 2); K3_MM(o1, w1, 3); SBAR();
        LBAR();
        K3_RUN12(o1, w1, 4, o0, w0, pO, pW, 0);
    }
    K3_RUN12(o0, w0, 0, o1, w1, pO, pW, 1);
    K3_MM(o0, w0, 12); K3_MM(o0, w0, 13); K3_MM(o0, w0, 14); K3_MM(o0, w0, 15);
#pragma unroll
    for (int n = 0; n < 16; ++n) K3_MM(o1, w1, (n >> 2) | ((n & 3) << 2));
    STAMP(3);
    {
        const __amdgpu_buffer_rsrc_t rOut = mk_rsrc(out, SEQ * 2 * DM * 2);
        const unsigned ob = ((rb * 128 + wr * 64 + c16) * (2 * DM) + br * DM + cb * 128 + wc * 64 + g * 8) * 2;
#pragma unroll
        for (int i = 0; i < 4; ++i)
#pragma unroll
            for (int u = 0; u < 2; ++u)
                st16<AUX_OUT>(rOut, ob + i * 16 * (2 * DM * 2) + u * 64, pack44(acc[i][2 * u], acc[i][2 * u + 1]));
    }
    STAMP_FLUSH(2);
    }
#undef K3_DW
#undef K3_DO
#undef K3_RO
#undef K3_RW
#undef K3_RWH
#undef K3_MR
#undef K3_MM
#undef K3_RUN12
#undef LBAR
}

__device__ __noinline__ __attribute__((used)) void code_mark_b() { asm volatile(""); }

constexpr int K1_ASLOT = 32768, K1_BSLOT = 12288, K1_BOFF = 3 * K1_ASLOT, K1_LDS = K1_BOFF + 5 * K1_BSLOT + 256;

DI int k1_head(int br, int hp, int sec, int hh) {
    const int off = sec == 0 ? 0 : (sec == 1 || br) ? 3 : 6;
    return ((hp & 8) + ((hp + off) & 7)) * 2 + hh;
}
__global__ __launch_bounds__(768) void qkv_kernel(const unsigned short* __restrict__ xA, const unsigned short* __restrict__ wA,
                                                  const unsigned short* __restrict__ xB, const unsigned short* __restrict__ wB,
                                                  char* __restrict__ ws) {
    extern __shared__ __attribute__((aligned(16))) char smem[];
    const lds_char* lds = (const lds_char*)smem;
    const unsigned lds0 = (unsigned)(size_t)lds;
    const int tid = threadIdx.x, lane = tid & 63;
    const int wid = rfl(tid >> 6), wm = wid & 3, hh = (wid >> 2) & 1;
    STAMP_DECL; STAMP(0);
    const int xg = blockIdx.x & 7, j = blockIdx.x >> 3;
    const int br = xg >> 2;
    const int mb = (xg & 1) * 4 + (j & 3);
    const int hp = ((xg >> 1) & 1) * 8 + (j >> 2);
#define NEXT_A(s) ((s) == 2 * K1_ASLOT ? 0u : (s) + K1_ASLOT)
#define NEXT_B(s) ((s) == 4 * K1_BSLOT ? 0u : (s) + K1_BSLOT)

    if (wid >= 8) {
        const unsigned short* X = br ? xB : xA;
        const unsigned short* W = br ? wB : wA;
        const __amdgpu_buffer_rsrc_t rX = mk_rsrc(X + (size_t)mb * 256 * DM, 256 * DM * 2);
        const __amdgpu_buffer_rsrc_t rW = mk_rsrc(W, DM * 3 * DM * 2);
        const int hw = wid - 8;
        unsigned voA[2], voB[3];
#pragma unroll
        for (int i = 0; i < 2; ++i) {
            const int R = hw * 64 + i * 8 + (lane >> 3);
            voA[i] = R * (DM * 2) + ((lane & 7) ^ ((R >> 1) & 7)) * 16;
        }
#pragma unroll
        for (int i = 0; i < 3; ++i) {
            const int blk = i * 4 + (lane >> 4), kg = hw * 2 + blk / 6, cb = blk % 6;
            const int hd = k1_head(br, hp, cb >> 1, cb & 1);
            voB[i] = (kg * 4 + ((lane >> 2) & 3)) * (3 * DM * 2) + ((cb >> 1) * DM + hd * 32 + ((lane & 3) ^ ((hw & 1) * 2)) * 8) * 2;
        }
        const unsigned dA = lds0 + hw * 8192, dB = lds0 + K1_BOFF + hw * 3072;
#define K1_DMA_A(t, slot) do { _Pragma("unroll") for (int i_ = 0; i_ < 8; ++i_) \
            DMA_X(rX, voA[i_ & 1], (unsigned)(t) * 128u + (i_ >> 1) * (16u * DM * 2), dA + (slot) + i_ * 1024); } while (0)
#define K1_DMA_B(u, slot) do { _Pragma("unroll") for (int i_ = 0; i_ < 3; ++i_) \
            DMA_W(rW, voB[i_], (unsigned)(u) * (32u * 3 * DM * 2), dB + (slot) + i_ * 1024); } while (0)
        K1_DMA_B(0, 0); K1_DMA_A(0, 0);
        K1_DMA_B(1, K1_BSLOT);
        WAIT_BAR(3);
        K1_DMA_B(2, 2 * K1_BSLOT); K1_DMA_A(1, K1_ASLOT);
        K1_DMA_B(3, 3 * K1_BSLOT);
        WAIT_BAR(14);
        K1_DMA_B(4, 4 * K1_BSLOT); K1_DMA_A(2, 2 * K1_ASLOT);
        K1_DMA_B(5, 0);
        WAIT_BAR(17);
        unsigned sa = 0, sb = K1_BSLOT;
#pragma unroll 1
        for (int t = 0; t < 13; ++t) {
            K1_DMA_B(2 * t + 6, sb); K1_DMA_A(t + 3, sa);
            sb = NEXT_B(sb); sa = NEXT_A(sa);
            WAIT_BAR(25);
            K1_DMA_B(2 * t + 7, sb);
            sb = NEXT_B(sb);
            WAIT_BAR(17);
        }
        WAIT_BAR(14);
        WAIT_BAR(3);
        WAIT_BAR(0);
        asm volatile("s_barrier" ::: "memory");
        if ((CODE_PF & 1) && hw == 0) code_touch((const void*)&code_mark_a, (const void*)&code_mark_b, lane, lds0 + K1_LDS - 256);
        return;
    }

    if (blockIdx.x == 0 && tid < 32) ((unsigned*)(ws + WS_CNT))[tid * 32] = 0u;
    if ((CODE_PF & 2) && wid == 7) code_touch((const void*)&code_mark_b, (const void*)&code_mark_c, lane, lds0 + K1_LDS - 256);
    const int c16 = lane & 15, g = lane >> 4;
    unsigned rdA[2], rdB[2];
#pragma unroll
    for (int u = 0; u < 2; ++u) {
        rdA[u] = (wm * 64 + c16) * 128 + (((4 * u + g) ^ ((c16 >> 1) & 7)) * 16);
        rdB[u] = K1_BOFF + g * 3072 + (c16 >> 2) * 64 + ((u ^ (g & 1)) * 32) + (lane & 3) * 8 + hh * 256;
    }
    f32x4 acc[4][6];
    {
        const float init = br ? 1e-6f : 0.f;
#pragma unroll
        for (int i = 0; i < 4; ++i)
#pragma unroll
            for (int c = 0; c < 6; ++c) { const float v = c < 4 ? init : 0.f; acc[i][c] = f32x4{v, v, v, v}; }
    }
    bf16x8 a0[4], a1[4];
    s16x4 bb[6][2];
#define RDA(A, i, u, sa) A[i] = lds_rd128(lds + (sa) + rdA[u] + (i) * 2048)
#define RDBH(c, t, sb) bb[c][t] = lds_tr(lds + (sb) + rdB[(c) & 1] + ((c) >> 1) * 512 + (t) * 1536)
#define RDB(c, sb) do { RDBH(c, 0, sb); RDBH(c, 1, sb); } while (0)
#define MM(A, i, c) do { \
        if ((c) < 2 || ((c) < 4 && !BR)) acc[i][c] = MFMA16(cat8(bb[c][0], bb[c][1]), A[i], acc[i][c]); \
        else acc[i][c] = MFMA16(A[i], cat8(bb[c][0], bb[c][1]), acc[i][c]); } while (0)
#define MR(A, i, c, RD) do { MM(A, i, c); SBAR(); RD; SBAR(); } while (0)
#define MM2(A, i, c) do { MM(A, i, c); MM(A, (i) + 1, c); } while (0)
#define LBAR() do { asm volatile("s_waitcnt lgkmcnt(0)\n\ts_barrier" ::: "memory"); SBAR(); } while (0)
#define PH_A(A, sb) do { \
        MR(A, 0, 0, RDBH(3, 0, sb)); MR(A, 1, 0, RDBH(3, 1, sb)); MR(A, 2, 0, RDBH(4, 0, sb)); MR(A, 3, 0, RDBH(4, 1, sb)); \
        MR(A, 0, 1, RDBH(5, 0, sb)); MR(A, 1, 1, RDBH(5, 1, sb)); MM2(A, 2, 1); MM2(A, 0, 2); MM2(A, 2, 2); SBAR(); } while (0)
#define PH_B(A, AN, un, san, sbn) do { \
        MM2(A, 0, 3); SBAR(); LBAR(); \
        MR(A, 2, 3, RDBH(0, 0, sbn)); MR(A, 3, 3, RDBH(0, 1, sbn)); MR(A, 0, 4, RDA(AN, 0, un, san)); MR(A, 1, 4, RDA(AN, 1, un, san)); \
        MR(A, 2, 4, RDA(AN, 2, un, san)); MR(A, 3, 4, RDA(AN, 3, un, san)); MR(A, 0, 5, RDBH(1, 0, sbn)); MR(A, 1, 5, RDBH(1, 1, sbn)); \
        MR(A, 2, 5, RDBH(2, 0, sbn)); MR(A, 3, 5, RDBH(2, 1, sbn)); } while (0)

    auto mainloop = [&](auto brc) {
        constexpr bool BR = decltype(brc)::value;
        STAMP(1);
        LBAR();
        STAMP(2);
        RDB(0, 0); RDA(a0, 0, 0, 0); RDA(a0, 1, 0, 0); RDA(a0, 2, 0, 0); RDA(a0, 3, 0, 0); RDB(1, 0); RDB(2, 0);
        __builtin_amdgcn_s_waitcnt(0xC07F);
        SBAR();
        unsigned sa = 0, sb = 0;
#pragma unroll 1
        for (int t = 0; t < 16; ++t) {
            const unsigned sb1 = NEXT_B(sb), sb2 = NEXT_B(sb1), sa1 = NEXT_A(sa);
            PH_A(a0, sb);
            PH_B(a0, a1, 1, sa, sb1);
            PH_A(a1, sb1);
            PH_B(a1, a0, 0, sa1, sb2);
            sa = sa1; sb = sb2;
        }
    int tz = threadIdx.x; asm volatile("" : "+v"(tz));
    const int tid = tz, c16 = tz & 15, g = (tz >> 4) & 3;
    STAMP(3);
    const int tile0 = mb * 8 + wm * 2;
    const unsigned po = ((g >> 1) * 64 + (g & 1) * 32) * 16;
    const __amdgpu_buffer_rsrc_t rWS = mk_rsrc(ws, 26u << 20);
    const int hq = k1_head(br, hp, 0, hh), hk = k1_head(br, hp, 1, hh);
    if constexpr (!BR) {
        const int hv = k1_head(0, hp, 2, hh);
#pragma unroll
        for (int m = 0; m < 2; ++m) {
#pragma unroll
            for (int a = 0; a < 2; ++a) {
                st16<AUX_A>(rWS, WS_KV + (hk * 64 + tile0 + m) * 4096 + po + ((c16 & 7) | (a << 3) | ((c16 >> 3) << 4)) * 16,
                     pack44(acc[2 * m + a][2], acc[2 * m + a][3]));
                st16<AUX_A>(rWS, WS_KV + (hv * 64 + tile0 + m) * 4096 + 2048 + po + (16 * a + c16) * 16, pack44(acc[2 * m][4 + a], acc[2 * m + 1][4 + a]));
                st16<AUX_A>(rWS, WS_Q + (hq * 64 + tile0 + m) * 2048 + po + (16 * a + c16) * 16,
                     pack44(acc[2 * m + a][0] * QSCALE, acc[2 * m + a][1] * QSCALE));
            }
        }
    } else {
        auto phi = [](f32x4& v) {
#pragma unroll
            for (int r = 0; r < 4; r += 2) {
                const f32x2 t = {v[r], v[r + 1]};
                const f32x2 u = t * LOG2E + (-1e-6f * LOG2E);
                const f32x2 e = {__builtin_amdgcn_fmed3f(__builtin_amdgcn_exp2f(u[0]), 0.f, 1.f), __builtin_amdgcn_fmed3f(__builtin_amdgcn_exp2f(u[1]), 0.f, 1.f)};
                const f32x2 mx = {__int_as_float(max(__float_as_int(t[0]), 0x358637bd)), __int_as_float(max(__float_as_int(t[1]), 0x358637bd))};
                const f32x2 o2 = mx + e;
                v[r] = o2[0]; v[r + 1] = o2[1];
            }
        };
        f32x4 kvt[2][2] = {}, zt[2] = {};
        bf16x8 ones;
#pragma unroll
        for (int e = 0; e < 8; ++e) ones[e] = (short)0x3F80;
#pragma unroll
        for (int m = 0; m < 2; ++m) {
            bf16x8 vf[2];
#pragma unroll
            for (int b = 0; b < 2; ++b) vf[b] = __builtin_bit_cast(bf16x8, pack44(acc[2 * m][4 + b], acc[2 * m + 1][4 + b]));
#pragma unroll
            for (int b = 0; b < 2; ++b) {
                phi(acc[2 * m][2 + b]); phi(acc[2 * m + 1][2 + b]);
                const bf16x8 pk = __builtin_bit_cast(bf16x8, pack44(acc[2 * m][2 + b], acc[2 * m + 1][2 + b]));
                kvt[b][0] = MFMA16(pk, vf[0], kvt[b][0]);
                kvt[b][1] = MFMA16(pk, vf[1], kvt[b][1]);
                zt[b] = MFMA16(pk, ones, zt[b]);
            }
        }
#pragma unroll
        for (int i = 0; i < 2; ++i) {
            phi(acc[i][0]); phi(acc[i][1]);
            st16<AUX_B>(rWS, WS_PQ + (hq * 64 + tile0) * 2048 + po + (16 * i + c16) * 16, pack44(acc[i][0], acc[i][1]));
        }
        lds_char* red = (lds_char*)smem + K1_ASLOT;
#pragma unroll
        for (int e = 0; e < 2; ++e) {
            lds_char* rp = red + wid * 4096 + (g >> 1) * 2048 + ((g & 1) * 32 + 16 * e + c16) * 8;
            *LDSP(f32x2, rp) = f32x2{kvt[0][e][0], kvt[0][e][1]};
            *LDSP(f32x2, rp + 512) = f32x2{kvt[0][e][2], kvt[0][e][3]};
            *LDSP(f32x2, rp + 1024) = f32x2{kvt[1][e][0], kvt[1][e][1]};
            *LDSP(f32x2, rp + 1536) = f32x2{kvt[1][e][2], kvt[1][e][3]};
        }
        if (c16 == 0) {
            lds_char* zp = red + 32768 + wid * 128 + (g & 1) * 64 + (g >> 1) * 32;
            *LDSP(f32x4, zp) = zt[0];
            *LDSP(f32x4, zp + 16) = zt[1];
        }
#pragma unroll
        for (int i = 2; i < 4; ++i) {
            phi(acc[i][0]); phi(acc[i][1]);
            st16<AUX_B>(rWS, WS_PQ + (hq * 64 + tile0 + 1) * 2048 + po + (16 * (i - 2) + c16) * 16, pack44(acc[i][0], acc[i][1]));
        }
        asm volatile("s_waitcnt lgkmcnt(0)\n\ts_barrier" ::: "memory");
        {
            const int idx = tid & 255;
            f32x4 s4 = *LDSP(const f32x4, red + (hh * 4 + 0) * 4096 + idx * 16);
#pragma unroll
            for (int m = 1; m < 4; ++m) s4 += *LDSP(const f32x4, red + (hh * 4 + m) * 4096 + idx * 16);
            st16<AUX_B>(rWS, WS_KVP + ((hk * 8 + mb) * 256 + idx) * 16, __builtin_bit_cast(u32x4, s4));
            if (idx < 32) {
                float z = 0.f;
#pragma unroll
                for (int m = 0; m < 4; ++m) z += *LDSP(const float, red + 32768 + (hh * 4 + m) * 128 + idx * 4);
                ((float*)(ws + WS_ZP))[(hk * 8 + mb) * 32 + idx] = z;
            }
        }
    }
    };
    if (br) mainloop(std::integral_constant<bool, true>{}); else mainloop(std::integral_constant<bool, false>{});
    STAMP_FLUSH(0);
#undef RDA
#undef RDB
#undef MM
#undef MM2
#undef MR
#undef RDBH
#undef PH_A
#undef PH_B
#undef LBAR
#undef K1_DMA_A
#undef K1_DMA_B
}

__device__ __noinline__ __attribute__((used)) void code_mark_c() { asm volatile(""); }

extern "C" void kernel_launch(void* const* d_in, const int* in_sizes, int n_in, void* d_out, int out_size, void* d_ws,
                              size_t ws_size, hipStream_t stream) {
    const unsigned short* xA = (const unsigned short*)d_in[0];
    const unsigned short* wqkvA = (const unsigned short*)d_in[1];
    const unsigned short* woA = (const unsigned short*)d_in[2];
    const unsigned short* xB = (const unsigned short*)d_in[3];
    const unsigned short* wqkvB = (const unsigned short*)d_in[4];
    const unsigned short* woB = (const unsigned short*)d_in[5];
    char* ws = (char*)d_ws;
    (void)hipFuncSetAttribute((const void*)qkv_kernel, hipFuncAttributeMaxDynamicSharedMemorySize, K1_LDS);
    (void)hipFuncSetAttribute((const void*)attn_kernel, hipFuncAttributeMaxDynamicSharedMemorySize, K23_LDS);
    qkv_kernel<<<256, 768, K1_LDS, stream>>>(xA, wqkvA, xB, wqkvB, ws);
    attn_kernel<<<256, 512, K23_LDS, stream>>>(ws, woA, woB, (unsigned short*)d_out);
}
```

```cpp
#include <hip/hip_runtime.h>
#include <hip/hip_bf16.h>

typedef __attribute__((ext_vector_type(8))) short bf16x8;
typedef __attribute__((ext_vector_type(4))) short s16x4;
typedef __attribute__((ext_vector_type(16))) float f32x16;
typedef __attribute__((ext_vector_type(4))) float f32x4;
typedef __attribute__((ext_vector_type(2))) float f32x2;
typedef __attribute__((ext_vector_type(4))) unsigned u32x4;
typedef __attribute__((address_space(3))) char lds_char;
#define LDSP(T, p) ((__attribute__((address_space(3))) T*)(p))

#define DI __device__ __forceinline__
#define MFMA32(a, b, c) __builtin_amdgcn_mfma_f32_32x32x16_bf16((a), (b), (c), 0, 0, 0)
#define MFMA16(a, b, c) __builtin_amdgcn_mfma_f32_16x16x32_bf16((a), (b), (c), 0, 0, 0)
#define SBAR() __builtin_amdgcn_sched_barrier(0)
#define PIN(x) asm volatile("" : "+v"(x))
#define WAIT_BAR(N) asm volatile("s_waitcnt vmcnt(" #N ") lgkmcnt(0)\n\ts_barrier" ::: "memory")

#define STAMP_DECL
#define STAMP(i) do {} while (0)
#define STAMP_FLUSH(kid) do {} while (0)
constexpr int SEQ = 2048, DM = 1024;
constexpr float LOG2E = 1.4426950408889634f;
constexpr float QSCALE = 0.17677669529663687f * LOG2E;

constexpr size_t WS_Q = 0;
constexpr size_t WS_KV = (size_t)4 << 20;
constexpr size_t WS_PQ = (size_t)12 << 20;
constexpr size_t WS_O = (size_t)16 << 20;
constexpr size_t WS_KVP = (size_t)24 << 20;
constexpr size_t WS_ZP = (size_t)25 << 20;
constexpr size_t WS_CNT = WS_ZP + 65536;

typedef __attribute__((ext_vector_type(2))) __bf16 bf16x2_t;
DI unsigned cvtpk(float lo, float hi) {
    return __builtin_bit_cast(unsigned, __builtin_convertvector((f32x2{lo, hi}), bf16x2_t));
}
DI bf16x8 pack8(const f32x16& x, int s) {
    u32x4 p;
    p[0] = cvtpk(x[8 * s + 0], x[8 * s + 1]);
    p[1] = cvtpk(x[8 * s + 2], x[8 * s + 3]);
    p[2] = cvtpk(x[8 * s + 4], x[8 * s + 5]);
    p[3] = cvtpk(x[8 * s + 6], x[8 * s + 7]);
    return __builtin_bit_cast(bf16x8, p);
}
DI void dma16(__amdgpu_buffer_rsrc_t rsrc, unsigned voff, unsigned soff, unsigned lds_dst) {
    unsigned keep;
    asm volatile("s_mov_b32 %0, m0\n\ts_mov_b32 m0, %4\n\ts_nop 0\n\tbuffer_load_dwordx4 %1, %2, %3 offen lds\n\ts_mov_b32 m0, %0"
                 : "=&s"(keep) : "v"(voff), "s"(rsrc), "s"(soff), "s"(lds_dst) : "memory");
}
DI void dma16_nt(__amdgpu_buffer_rsrc_t rsrc, unsigned voff, unsigned soff, unsigned lds_dst) {
    unsigned keep;
    asm volatile("s_mov_b32 %0, m0\n\ts_mov_b32 m0, %4\n\ts_nop 0\n\tbuffer_load_dwordx4 %1, %2, %3 offen nt lds\n\ts_mov_b32 m0, %0"
                 : "=&s"(keep) : "v"(voff), "s"(rsrc), "s"(soff), "s"(lds_dst) : "memory");
}
DI void dma16_sc1(__amdgpu_buffer_rsrc_t rsrc, unsigned voff, unsigned soff, unsigned lds_dst) {
    unsigned keep;
    asm volatile("s_mov_b32 %0, m0\n\ts_mov_b32 m0, %4\n\ts_nop 0\n\tbuffer_load_dwordx4 %1, %2, %3 offen sc1 lds\n\ts_mov_b32 m0, %0"
                 : "=&s"(keep) : "v"(voff), "s"(rsrc), "s"(soff), "s"(lds_dst) : "memory");
}
DI __amdgpu_buffer_rsrc_t mk_rsrc(const void* p, unsigned bytes) {
    return __builtin_amdgcn_make_buffer_rsrc((void*)p, (short)0, (int)bytes, 0x00020000);
}
constexpr int SC1 = 16;
template <int AUX> DI void st16(__amdgpu_buffer_rsrc_t rsrc, unsigned voff, u32x4 v) { __builtin_amdgcn_raw_buffer_store_b128(v, rsrc, voff, 0, AUX); }
template <int AUX> DI void st16(__amdgpu_buffer_rsrc_t rsrc, unsigned voff, bf16x8 v) { st16<AUX>(rsrc, voff, __builtin_bit_cast(u32x4, v)); }
#define ASM_LD16(dst, rsrc, voff, soff) asm volatile("buffer_load_dwordx4 %0, %1, %2, %3 offen" : "=v"(dst) : "v"(voff), "s"(rsrc), "s"(soff) : "memory")
#define ASM_LD8(dst, rsrc, voff, soff) asm volatile("buffer_load_dwordx2 %0, %1, %2, %3 offen" : "=v"(dst) : "v"(voff), "s"(rsrc), "s"(soff) : "memory")
DI bf16x8 lds_rd128(const lds_char* p) { return *LDSP(const bf16x8, p); }
DI s16x4 lds_tr(const lds_char* p) {
    typedef short v4i16_t __attribute__((ext_vector_type(4)));
    return __builtin_bit_cast(s16x4, __builtin_amdgcn_ds_read_tr16_b64_v4i16(LDSP(v4i16_t, p)));
}
DI bf16x8 cat8(s16x4 lo, s16x4 hi) { return __builtin_shufflevector(lo, hi, 0, 1, 2, 3, 4, 5, 6, 7); }
DI unsigned rfl(unsigned x) { return __builtin_amdgcn_readfirstlane(x); }

__device__ __noinline__ __attribute__((used)) void code_mark_a() { asm volatile(""); }
__device__ __noinline__ __attribute__((used)) void code_mark_b();
__device__ __noinline__ __attribute__((used)) void code_mark_c();
DI void line_touch(const void* beg, const void* end, int lane, unsigned lds_sink) {
    const long len = (const char*)end - (const char*)beg;
    const __amdgpu_buffer_rsrc_t r = mk_rsrc(beg, len > 0 && len <= 16384 * 64 ? (unsigned)len : 0u);
    const unsigned vo = lane * 128;
    unsigned keep;
    asm volatile("s_mov_b32 %0, m0\n\ts_mov_b32 m0, %4\n\ts_nop 4\n\tbuffer_load_dword %1, %2, 0 offen lds\n\tbuffer_load_dword %1, %2, %3 offen lds\n\ts_mov_b32 m0, %0"
                 : "=&s"(keep) : "v"(vo), "s"(r), "s"(8192u), "s"(lds_sink) : "memory");
}

DI u32x4 pack44(const f32x4& a, const f32x4& b) {
    u32x4 p;
    p[0] = cvtpk(a[0], a[1]); p[1] = cvtpk(a[2], a[3]); p[2] = cvtpk(b[0], b[1]); p[3] = cvtpk(b[2], b[3]);
    return p;
}

constexpr int K2_SLOT = 16384, K2_XOFF = 8 * K2_SLOT;
constexpr unsigned K2_BARS = 0x1117;

DI float xhalf_max(float m) {
    auto rr = __builtin_amdgcn_permlane32_swap(__float_as_uint(m), __float_as_uint(m), false, false);
    return __builtin_fmaxf(__uint_as_float(rr[0]), __uint_as_float(rr[1]));
}
DI float xhalf_sum(float m) {
    auto rr = __builtin_amdgcn_permlane32_swap(__float_as_uint(m), __float_as_uint(m), false, false);
    return __uint_as_float(rr[0]) + __uint_as_float(rr[1]);
}
DI float max16(const f32x16& s) {
    float a = __builtin_fmaxf(s[0], s[1]);
#pragma unroll
    for (int r = 2; r < 16; ++r) a = __builtin_fmaxf(a, s[r]);
    return a;
}

constexpr int K3_KG = 1032, K3_WSLOT = 16 * K3_KG, K3_OOFF = 4 * K3_WSLOT, K3_OSLOT = 16384, K23_LDS = K3_OOFF + 5 * K3_OSLOT;
#define NEXT_O(s) ((s) == 4 * K3_OSLOT ? 0u : (s) + K3_OSLOT)

__global__ __launch_bounds__(512, 2) void attn_kernel(char* __restrict__ ws, const unsigned short* __restrict__ woA,
                                                      const unsigned short* __restrict__ woB, unsigned short* __restrict__ out) {
    extern __shared__ __attribute__((aligned(16))) char smem[];
    const lds_char* lds = (const lds_char*)smem;
    const unsigned lds0 = (unsigned)(size_t)lds;
    const int tid = threadIdx.x, lane = tid & 63;
    const int wid = rfl(tid >> 6);
    const int hi = lane >> 5;
    STAMP_DECL; STAMP(0);
    const int xg = blockIdx.x & 7, j0 = blockIdx.x >> 3;
    const int head = xg * 4 + (j0 >> 3), qb = j0 & 7;
    const int br = xg >> 2, rb = (xg & 3) * 4 + (j0 >> 3), cb = j0 & 7;
    unsigned* cnt = (unsigned*)(ws + WS_CNT);
    const __amdgpu_buffer_rsrc_t rWS = mk_rsrc(ws, 26u << 20);
    const __amdgpu_buffer_rsrc_t rKV = mk_rsrc(ws + WS_KV + (size_t)head * 64 * 4096, 64 * 4096);
    const unsigned vl16 = lane * 16;
#define BF(x) __builtin_bit_cast(bf16x8, x)

    if (wid >= 4) {
        const int hw = wid - 4;
        const unsigned vo = hw * 4096 + vl16, dKV = lds0 + hw * 4096;
#define K2_DMA(st) do { const unsigned d_ = dKV + ((st) & 7) * K2_SLOT; \
            dma16(rKV, vo, (unsigned)(st) * K2_SLOT + 4096u, d_); dma16(rKV, vo, (unsigned)(st) * K2_SLOT + 4096u + 1024u, d_ + 1024); \
            dma16(rKV, vo, (unsigned)(st) * K2_SLOT + 4096u + 2048u, d_ + 2048); dma16(rKV, vo, (unsigned)(st) * K2_SLOT + 3072u, d_ + 3072); } while (0)
        K2_DMA(0); K2_DMA(1);
        WAIT_BAR(4);
        K2_DMA(2); K2_DMA(3);
        WAIT_BAR(8);
        K2_DMA(4); K2_DMA(5);
        WAIT_BAR(8);
        K2_DMA(6); K2_DMA(7);
        f32x2 kp[2][8];
        f32x4 zp[8];
        bf16x8 pq[4];
        {
            const unsigned vk = lane * 8, vz = hi * 64;
#pragma unroll
            for (int m = 0; m < 8; ++m) {
                ASM_LD8(kp[0][m], rWS, vk, (unsigned)(WS_KVP + (head * 8 + m) * 4096 + hw * 1024));
                ASM_LD8(kp[1][m], rWS, vk, (unsigned)(WS_KVP + (head * 8 + m) * 4096 + hw * 1024 + 512));
                ASM_LD16(zp[m], rWS, vz, (unsigned)(WS_ZP + (head * 8 + m) * 128 + hw * 16));
            }
#pragma unroll
            for (int i = 0; i < 4; ++i) ASM_LD16(pq[i], rWS, vl16, (unsigned)(WS_PQ + (head * 64 + qb * 8 + hw * 2) * 2048 + i * 1024));
        }
        WAIT_BAR(28);
        K2_DMA(8); K2_DMA(9); K2_DMA(10); K2_DMA(11);
        asm volatile("s_waitcnt vmcnt(16)"
                     : "+v"(kp[0][0]), "+v"(kp[0][1]), "+v"(kp[0][2]), "+v"(kp[0][3]), "+v"(kp[0][4]), "+v"(kp[0][5]), "+v"(kp[0][6]), "+v"(kp[0][7]),
                       "+v"(kp[1][0]), "+v"(kp[1][1]), "+v"(kp[1][2]), "+v"(kp[1][3]), "+v"(kp[1][4]), "+v"(kp[1][5]), "+v"(kp[1][6]), "+v"(kp[1][7]) :: "memory");
        asm volatile("" : "+v"(zp[0]), "+v"(zp[1]), "+v"(zp[2]), "+v"(zp[3]), "+v"(zp[4]), "+v"(zp[5]), "+v"(zp[6]), "+v"(zp[7]),
                          "+v"(pq[0]), "+v"(pq[1]), "+v"(pq[2]), "+v"(pq[3]));
        {
            f32x2 a0 = kp[0][0], a1 = kp[1][0];
            f32x4 z = zp[0];
#pragma unroll
            for (int m = 1; m < 8; ++m) { a0 += kp[0][m]; a1 += kp[1][m]; z += zp[m]; }
            typedef __attribute__((ext_vector_type(2))) unsigned u32x2;
            lds_char* xs = (lds_char*)smem + K2_XOFF + (hw >> 1) * 1024 + vl16 + (hw & 1) * 8;
            *LDSP(u32x2, xs) = u32x2{cvtpk(a0[0], a0[1]), cvtpk(a1[0], a1[1])};
            *LDSP(u32x2, xs + 2048) = u32x2{cvtpk(z[0], z[1]), cvtpk(z[2], z[3])};
        }
        WAIT_BAR(0);
        K2_DMA(12); K2_DMA(13); K2_DMA(14); K2_DMA(15);
        {
            const char* wb = (const char*)((xg >> 2) ? woB : woA) + j0 * 65536 + hw * 16384;
            line_touch(wb, wb + 16384, lane, lds0 + K2_XOFF + 4096);
        }
        {
            const lds_char* xs = lds + K2_XOFF + vl16;
            const bf16x8 kv0 = lds_rd128(xs), kv1 = lds_rd128(xs + 1024), z0 = lds_rd128(xs + 2048), z1 = lds_rd128(xs + 3072);
#pragma unroll
            for (int u = 0; u < 2; ++u) {
                f32x16 num = {}, den = {};
                num = MFMA32(kv0, pq[2 * u], num); den = MFMA32(z0, pq[2 * u], den);
                num = MFMA32(kv1, pq[2 * u + 1], num); den = MFMA32(z1, pq[2 * u + 1], den);
                const float rd = 1.0f / (den[0] + 1e-6f);
                num *= rd;
                const unsigned ob = WS_O + ((64 + qb * 8 + hw * 2 + u) * 32 + head) * 2048 + vl16;
                st16<SC1>(rWS, ob, pack8(num, 0));
                st16<SC1>(rWS, ob + 1024, pack8(num, 1));
            }
        }
        asm volatile("s_waitcnt vmcnt(%0) lgkmcnt(0)\n\ts_barrier" :: "i"(2 + 4) : "memory");
        asm volatile("s_waitcnt vmcnt(0)" ::: "memory");
        if (lane == 0) __hip_atomic_fetch_add(cnt + (16 + qb * 2 + (hw >> 1)) * 32, 1u, __ATOMIC_RELAXED, __HIP_MEMORY_SCOPE_AGENT);

        const __amdgpu_buffer_rsrc_t rW3 = mk_rsrc(br ? woB : woA, DM * DM * 2);
        const __amdgpu_buffer_rsrc_t rO = mk_rsrc(ws + WS_O + (size_t)(br * 64 + rb * 4 + hw) * 65536, 65536);
        const unsigned voW = (hw * 16 + ((lane >> 2) & 3)) * (DM * 2) + (cb * 128 + (lane >> 4) * 32 + (lane & 3) * 8) * 2;
        const unsigned dW = lds0 + hw * 4 * K3_KG, dO = lds0 + K3_OOFF + hw * 4096;
#define K3_DW(i) do { _Pragma("unroll") for (int n_ = 0; n_ < 4; ++n_) \
            dma16(rW3, voW, (unsigned)(i) * (64u * DM * 2) + n_ * (4u * DM * 2), dW + ((i) & 3) * K3_WSLOT + n_ * K3_KG); } while (0)
#define K3_DO(i, so) do { _Pragma("unroll") for (int n_ = 0; n_ < 4; ++n_) \
            dma16_sc1(rO, vl16, (unsigned)(i) * 4096u + n_ * 1024u, dO + (so) + n_ * 1024); } while (0)
        K3_DW(0); K3_DW(1); K3_DW(2);
        asm volatile("s_barrier" ::: "memory");
        K3_DW(3);
        if (hw == 0) {
            const unsigned* c = cnt + (br * 16 + rb) * 32;
            for (unsigned spin = 0; spin < (1u << 22); ++spin) {
                if (rfl(__hip_atomic_load(c, __ATOMIC_RELAXED, __HIP_MEMORY_SCOPE_AGENT)) >= 64u) break;
                __builtin_amdgcn_s_sleep(6);
            }
        }
        asm volatile("s_barrier" ::: "memory");
        K3_DO(0, 0); K3_DO(1, K3_OSLOT); K3_DO(2, 2 * K3_OSLOT); K3_DO(3, 3 * K3_OSLOT); K3_DO(4, 4 * K3_OSLOT);
        WAIT_BAR(16);
        WAIT_BAR(12);
        K3_DW(4); K3_DO(5, 0);
        WAIT_BAR(16);
        unsigned so = K3_OSLOT;
#pragma unroll 1
        for (int i = 2; i < 12; ++i) {
            K3_DW(i + 3); K3_DO(i + 4, so);
            so = NEXT_O(so);
            WAIT_BAR(20);
        }
        K3_DW(15);
        WAIT_BAR(16);
        WAIT_BAR(8);
        WAIT_BAR(0);
        return;
    }

    const int qt0 = qb * 8 + wid * 2;
    bf16x8 qa0, qa1, qb0, qb1, kx0, kx1, ky0, ky1, v0, v1;
    {
        const unsigned sq = WS_Q + (head * 64 + qt0) * 2048;
        ASM_LD16(qa0, rWS, vl16, sq); ASM_LD16(qa1, rWS, vl16, sq + 1024);
        ASM_LD16(kx0, rKV, vl16, 0u); ASM_LD16(kx1, rKV, vl16, 1024u);
        ASM_LD16(qb0, rWS, vl16, sq + 2048); ASM_LD16(qb1, rWS, vl16, sq + 3072);
        ASM_LD16(v0, rKV, vl16, 2048u);
    }
    bf16x8 sel;
    {
        const short v = (((lane >> 4) & 1) == (((lane & 15) >> 2) & 1)) ? (short)0x3F80 : (short)0;
#pragma unroll
        for (int e = 0; e < 8; ++e) sel[e] = v;
    }
    const lds_char* fb = lds + vl16;
    f32x16 sA, sB, nmA, nmB, oA = {}, oB = {};
    f32x4 lA = {}, lB = {};
    u32x4 p0, p1 = {};
    PIN(oA); PIN(oB); PIN(lA); PIN(lB); PIN(p1); PIN(sel);
    STAMP(1);
    asm volatile("s_waitcnt vmcnt(0)" : "+v"(qa0), "+v"(qa1), "+v"(qb0), "+v"(qb1), "+v"(kx0), "+v"(kx1), "+v"(v0) :: "memory");
    STAMP(2);
    {
        f32x16 z16 = {};
        sA = MFMA32(kx0, qa0, z16); sB = MFMA32(kx0, qb0, z16);
        sA = MFMA32(kx1, qa1, sA); sB = MFMA32(kx1, qb1, sB);
        const float ma = xhalf_max(max16(sA)), mb = xhalf_max(max16(sB));
#pragma unroll
        for (int r = 0; r < 16; ++r) { nmA[r] = -ma; nmB[r] = -mb; sA[r] -= ma; }
    }
    PIN(nmA); PIN(nmB);
    v1 = v0;

#define EX(X, i) X[i] = __builtin_amdgcn_exp2f(X[i])
#define CV(P, n, X, i) P[n] = cvtpk(X[i], X[(i) + 1])
#define X1(op) do { op; SBAR(); } while (0)
#define ATT(SC, SN, K0, K1, Q0, Q1, NM, VP, OP, LP, VC, OC, LC, R1, R2, R3) do { \
        X1(SN = MFMA32(K0, Q0, NM)); X1(R1); \
        X1(EX(SC, 0)); X1(EX(SC, 1)); X1(p1[3] = cvtpk(e14, e15)); X1(EX(SC, 2)); \
        X1(SN = MFMA32(K1, Q1, SN)); X1(R2); \
        X1(EX(SC, 3)); X1(CV(p0, 0, SC, 0)); X1(EX(SC, 4)); X1(EX(SC, 5)); \
        X1(OP = MFMA32(VP, BF(p1), OP)); X1(R3); \
        X1(CV(p0, 1, SC, 2)); X1(EX(SC, 6)); X1(EX(SC, 7)); X1(EX(SC, 8)); \
        X1(LP = MFMA16(sel, BF(p1), LP)); \
        X1(CV(p0, 2, SC, 4)); X1(EX(SC, 9)); X1(CV(p0, 3, SC, 6)); X1(EX(SC, 10)); X1(EX(SC, 11)); \
        X1(OC = MFMA32(VC, BF(p0), OC)); \
        X1(CV(p1, 0, SC, 8)); X1(EX(SC, 12)); X1(EX(SC, 13)); X1(e14 = __builtin_amdgcn_exp2f(SC[14])); \
        X1(LC = MFMA16(sel, BF(p0), LC)); \
        X1(CV(p1, 1, SC, 10)); X1(e15 = __builtin_amdgcn_exp2f(SC[15])); X1(CV(p1, 2, SC, 12)); \
        } while (0)
#define TILE2(KC0, KC1, V0C, V1P, KN0, KN1, V0N, V1C, TP) do { \
        ATT(sA, sB, KC0, KC1, qb0, qb1, nmB, V1P, oB, lB, V0C, oA, lA, \
            (V0N = lds_rd128((TP) + 2048), V1C = lds_rd128((TP) + 3072)), KN1 = lds_rd128((TP) + 1024), KN0 = lds_rd128(TP)); \
        ATT(sB, sA, KN0, KN1, qa0, qa1, nmA, V1C, oA, lA, V0C, oB, lB, (void)0, (void)0, (void)0); } while (0)
    bf16x8 vy0, vy1;
    float e14 = 0.f, e15 = 0.f;

    STAMP(3);
#pragma unroll 1
    for (int k = 0; k < 16; ++k) {
        if ((K2_BARS >> k) & 1) asm volatile("s_waitcnt lgkmcnt(0)\n\ts_barrier" ::: "memory");
        SBAR();
        const lds_char* cur = fb + (k & 7) * K2_SLOT;
        TILE2(kx0, kx1, v0, v1, ky0, ky1, vy0, vy1, cur);
        TILE2(ky0, ky1, vy0, vy1, kx0, kx1, v0, v1, cur + 4096);
        TILE2(kx0, kx1, v0, v1, ky0, ky1, vy0, vy1, cur + 2 * 4096);
        TILE2(ky0, ky1, vy0, vy1, kx0, kx1, v0, v1, cur + 3 * 4096);
    }
    p1[3] = cvtpk(e14, e15);
    oB = MFMA32(v1, BF(p1), oB);
    lB = MFMA16(sel, BF(p1), lB);
    STAMP(4);
    auto finish = [&](f32x16 o, float l, const bf16x8 q0, const bf16x8 q1, int qt) {
        float t = 0.f;
        bool nan_o = false;
#pragma unroll
        for (int r = 0; r < 16; ++r) { t = __builtin_fmaxf(t, __builtin_fabsf(o[r])); nan_o |= (o[r] != o[r]); }
        const bool bad = !(l < 1.8446744e19f) || !(t < 3.0e38f) || nan_o;
        if (__builtin_expect(__any(bad), 0)) {
            const u32x4* KV = (const u32x4*)(ws + WS_KV) + (size_t)head * 64 * 256 + lane;
            float m = -INFINITY, ls = 0.f;
#pragma unroll
            for (int r = 0; r < 16; ++r) o[r] = 0.f;
#pragma unroll 1
            for (int jt = 0; jt < 64; ++jt) {
                const bf16x8 a0 = BF(KV[jt * 256]), a1 = BF(KV[jt * 256 + 64]), b0 = BF(KV[jt * 256 + 128]), b1 = BF(KV[jt * 256 + 192]);
                f32x16 sc = {};
                sc = MFMA32(a0, q0, sc); sc = MFMA32(a1, q1, sc);
                const float mn = __builtin_fmaxf(m, xhalf_max(max16(sc)));
                const float al = __builtin_amdgcn_exp2f(m - mn);
                m = mn; ls *= al;
#pragma unroll
                for (int r = 0; r < 16; ++r) { o[r] *= al; sc[r] = __builtin_amdgcn_exp2f(sc[r] - mn); ls += sc[r]; }
                o = MFMA32(b0, pack8(sc, 0), o); o = MFMA32(b1, pack8(sc, 1), o);
            }
            l = xhalf_sum(ls);
        }
        o *= 1.0f / l;
        const unsigned ob = WS_O + (qt * 32 + head) * 2048 + vl16;
        st16<SC1>(rWS, ob, pack8(o, 0));
        st16<SC1>(rWS, ob + 1024, pack8(o, 1));
    };
    finish(oA, lA[0], qa0, qa1, qt0);
    finish(oB, lB[0], qb0, qb1, qt0 + 1);
    asm volatile("s_waitcnt vmcnt(0)" ::: "memory");
    if (lane == 0) __hip_atomic_fetch_add(cnt + (qb * 2 + (wid >> 1)) * 32, 1u, __ATOMIC_RELAXED, __HIP_MEMORY_SCOPE_AGENT);
#undef EX
#undef ATT
#undef CV
#undef TILE2
#undef X1
#undef K2_DMA

    {
    STAMP_DECL; STAMP(0);
    const int wr = wid & 1, wc = wid >> 1, c16 = lane & 15, g = lane >> 4;
    const unsigned rdO = K3_OOFF + wr * 8192 + (g >> 1) * 1024 + ((g & 1) * 32 + c16) * 16;
    const unsigned rdW = ((g >> 1) * 4 + (g & 1)) * K3_KG + wc * 512 + (c16 >> 2) * 64 + (lane & 3) * 16;
    f32x4 acc[4][4] = {};
    bf16x8 o0[4], o1[4];
    s16x4 w0[4][2], w1[4][2];
#define K3_RO(O, i, pO, hd) O[i] = lds_rd128((pO) + ((i) >> 1) * 4096 + ((i) & 1) * 256 + (hd) * 2048)
#define K3_RWH(Wf, ct, t, pW, hd) Wf[ct][t] = lds_tr((pW) + (hd) * 8 * K3_KG + (t) * 2 * K3_KG + ((ct) >> 1) * 256 + ((ct) & 1) * 8)
#define K3_RW(Wf, ct, pW, hd) do { K3_RWH(Wf, ct, 0, pW, hd); K3_RWH(Wf, ct, 1, pW, hd); } while (0)
#define K3_MM(OC, WC, n) acc[(n) & 3][(n) >> 2] = MFMA16(cat8(WC[(n) >> 2][0], WC[(n) >> 2][1]), OC[(n) & 3], acc[(n) & 3][(n) >> 2])
#define K3_MR(OC, WC, n, RD) do { K3_MM(OC, WC, n); SBAR(); RD; SBAR(); } while (0)
#define K3_RUN12(OC, WC, n0, ON, WN, pO, pW, hd) do { \
        K3_MR(OC, WC, (n0) + 0, K3_RWH(WN, 0, 0, pW, hd)); K3_MR(OC, WC, (n0) + 1, K3_RWH(WN, 0, 1, pW, hd)); \
        K3_MR(OC, WC, (n0) + 2, K3_RO(ON, 0, pO, hd));     K3_MR(OC, WC, (n0) + 3, K3_RO(ON, 1, pO, hd)); \
        K3_MR(OC, WC, (n0) + 4, K3_RO(ON, 2, pO, hd));     K3_MR(OC, WC, (n0) + 5, K3_RO(ON, 3, pO, hd)); \
        K3_MR(OC, WC, (n0) + 6, K3_RWH(WN, 1, 0, pW, hd)); K3_MR(OC, WC, (n0) + 7, K3_RWH(WN, 1, 1, pW, hd)); \
        K3_MR(OC, WC, (n0) + 8, K3_RWH(WN, 2, 0, pW, hd)); K3_MR(OC, WC, (n0) + 9, K3_RWH(WN, 2, 1, pW, hd)); \
        K3_MR(OC, WC, (n0) + 10, K3_RWH(WN, 3, 0, pW, hd)); K3_MR(OC, WC, (n0) + 11, K3_RWH(WN, 3, 1, pW, hd)); } while (0)
#define LBAR() do { asm volatile("s_waitcnt lgkmcnt(0)\n\ts_barrier" ::: "memory"); SBAR(); } while (0)
    LBAR();
    LBAR();
    STAMP(1);
    LBAR();
    STAMP(2);
    const lds_char* pO = lds + rdO;
    const lds_char* pW = lds + rdW;
    K3_RW(w0, 0, pW, 0); K3_RO(o0, 0, pO, 0); K3_RO(o0, 1, pO, 0); K3_RO(o0, 2, pO, 0); K3_RO(o0, 3, pO, 0); K3_RW(w0, 1, pW, 0); K3_RW(w0, 2, pW, 0); K3_RW(w0, 3, pW, 0);
    __builtin_amdgcn_s_waitcnt(0xC07F);
    SBAR();
    unsigned so = 0, sw = 0;
#pragma unroll 1
    for (int i = 0; i < 15; ++i) {
        K3_RUN12(o0, w0, 0, o1, w1, pO, pW, 1);
        K3_MR(o0, w0, 12, so = NEXT_O(so));
        K3_MR(o0, w0, 13, sw = (sw == 3 * K3_WSLOT ? 0u : sw + K3_WSLOT));
        K3_MR(o0, w0, 14, pO = lds + rdO + so; PIN(pO));
        K3_MR(o0, w0, 15, pW = lds + rdW + sw; PIN(pW));
        K3_MM(o1, w1, 0); K3_MM(o1, w1, 1); K3_MM(o1, w1, 2); K3_MM(o1, w1, 3); SBAR();
        LBAR();
        K3_RUN12(o1, w1, 4, o0, w0, pO, pW, 0);
    }
    K3_RUN12(o0, w0, 0, o1, w1, pO, pW, 1);
    K3_MM(o0, w0, 12); K3_MM(o0, w0, 13); K3_MM(o0, w0, 14); K3_MM(o0, w0, 15);
#pragma unroll
    for (int n = 0; n < 16; ++n) K3_MM(o1, w1, (n >> 2) | ((n & 3) << 2));
    STAMP(3);
    {
        const __amdgpu_buffer_rsrc_t rOut = mk_rsrc(out, SEQ * 2 * DM * 2);
        const unsigned ob = ((rb * 128 + wr * 64 + c16) * (2 * DM) + br * DM + cb * 128 + wc * 64 + g * 8) * 2;
#pragma unroll
        for (int i = 0; i < 4; ++i)
#pragma unroll
            for (int u = 0; u < 2; ++u)
                st16<SC1>(rOut, ob + i * 16 * (2 * DM * 2) + u * 64, pack44(acc[i][2 * u], acc[i][2 * u + 1]));
    }
    STAMP_FLUSH(2);
    }
#undef K3_DW
#undef K3_DO
#undef K3_RO
#undef K3_RW
#undef K3_RWH
#undef K3_MR
#undef K3_MM
#undef K3_RUN12
#undef LBAR
}

__device__ __noinline__ __attribute__((used)) void code_mark_b() { asm volatile(""); }

constexpr int K1_ASLOT = 32768, K1_BSLOT = 12288, K1_BOFF = 3 * K1_ASLOT, K1_LDS = K1_BOFF + 5 * K1_BSLOT + 256;

DI int k1_head(int br, int hp, int sec, int hh) {
    const int off = sec == 0 ? 0 : (sec == 1 || br) ? 1 : 2;
    return ((hp & 12) + ((hp + off) & 3)) * 2 + hh;
}
__global__ __launch_bounds__(768) void qkv_kernel(const unsigned short* __restrict__ xA, const unsigned short* __restrict__ wA,
                                                  const unsigned short* __restrict__ xB, const unsigned short* __restrict__ wB,
                                                  char* __restrict__ ws) {
    extern __shared__ __attribute__((aligned(16))) char smem[];
    const lds_char* lds = (const lds_char*)smem;
    const unsigned lds0 = (unsigned)(size_t)lds;
    const int tid = threadIdx.x, lane = tid & 63;
    const int wid = rfl(tid >> 6), wm = wid & 3, hh = (wid >> 2) & 1;
    STAMP_DECL; STAMP(0);
    const int xg = blockIdx.x & 7, j = blockIdx.x >> 3;
    const int br = (j >> 2) & 1;
    const int mb = (xg & 1) * 4 + (j & 3);
    const int hp = (xg >> 1) * 4 + (j >> 3);
#define NEXT_A(s) ((s) == 2 * K1_ASLOT ? 0u : (s) + K1_ASLOT)
#define NEXT_B(s) ((s) == 4 * K1_BSLOT ? 0u : (s) + K1_BSLOT)

    if (wid >= 8) {
        const unsigned short* X = br ? xB : xA;
        const unsigned short* W = br ? wB : wA;
        const __amdgpu_buffer_rsrc_t rX = mk_rsrc(X + (size_t)mb * 256 * DM, 256 * DM * 2);
        const __amdgpu_buffer_rsrc_t rW = mk_rsrc(W, DM * 3 * DM * 2);
        const int hw = wid - 8;
        unsigned voA[2], voB[3];
#pragma unroll
        for (int i = 0; i < 2; ++i) {
            const int R = hw * 64 + i * 8 + (lane >> 3);
            voA[i] = R * (DM * 2) + ((lane & 7) ^ ((R >> 1) & 7)) * 16;
        }
#pragma unroll
        for (int i = 0; i < 3; ++i) {
            const int blk = i * 4 + (lane >> 4), kg = hw * 2 + blk / 6, cb = blk % 6;
            const int hd = k1_head(br, hp, cb >> 1, cb & 1);
            voB[i] = (kg * 4 + ((lane >> 2) & 3)) * (3 * DM * 2) + ((cb >> 1) * DM + hd * 32 + ((lane & 3) ^ ((hw & 1) * 2)) * 8) * 2;
        }
        const unsigned dA = lds0 + hw * 8192, dB = lds0 + K1_BOFF + hw * 3072;
#define K1_DMA_A(t, slot) do { _Pragma("unroll") for (int i_ = 0; i_ < 8; ++i_) \
            dma16(rX, voA[i_ & 1], (unsigned)(t) * 128u + (i_ >> 1) * (16u * DM * 2), dA + (slot) + i_ * 1024); } while (0)
#define K1_DMA_B(u, slot) do { _Pragma("unroll") for (int i_ = 0; i_ < 3; ++i_) \
            dma16_nt(rW, voB[i_], (unsigned)(u) * (32u * 3 * DM * 2), dB + (slot) + i_ * 1024); } while (0)
        K1_DMA_B(0, 0); K1_DMA_A(0, 0);
        K1_DMA_B(1, K1_BSLOT);
        WAIT_BAR(3);
        K1_DMA_B(2, 2 * K1_BSLOT); K1_DMA_A(1, K1_ASLOT);
        K1_DMA_B(3, 3 * K1_BSLOT);
        WAIT_BAR(14);
        K1_DMA_B(4, 4 * K1_BSLOT); K1_DMA_A(2, 2 * K1_ASLOT);
        K1_DMA_B(5, 0);
        WAIT_BAR(17);
        unsigned sa = 0, sb = K1_BSLOT;
#pragma unroll 1
        for (int t = 0; t < 13; ++t) {
            K1_DMA_B(2 * t + 6, sb); K1_DMA_A(t + 3, sa);
            sb = NEXT_B(sb); sa = NEXT_A(sa);
            WAIT_BAR(25);
            K1_DMA_B(2 * t + 7, sb);
            sb = NEXT_B(sb);
            WAIT_BAR(17);
        }
        WAIT_BAR(14);
        WAIT_BAR(3);
        WAIT_BAR(0);
        asm volatile("s_barrier" ::: "memory");
        if (hw == 0) line_touch((const void*)&code_mark_a, (const void*)&code_mark_b, lane, lds0 + K1_LDS - 256);
        return;
    }

    if (blockIdx.x == 0 && tid < 32) ((unsigned*)(ws + WS_CNT))[tid * 32] = 0u;
    if (wid == 7) line_touch((const void*)&code_mark_b, (const void*)&code_mark_c, lane, lds0 + K1_LDS - 256);
    const int c16 = lane & 15, g = lane >> 4;
    unsigned rdA[2], rdB[2];
#pragma unroll
    for (int u = 0; u < 2; ++u) {
        rdA[u] = (wm * 64 + c16) * 128 + (((4 * u + g) ^ ((c16 >> 1) & 7)) * 16);
        rdB[u] = K1_BOFF + g * 3072 + (c16 >> 2) * 64 + ((u ^ (g & 1)) * 32) + (lane & 3) * 8 + hh * 256;
    }
    f32x4 acc[4][6];
    {
        const float init = br ? 1e-6f : 0.f;
#pragma unroll
        for (int i = 0; i < 4; ++i)
#pragma unroll
            for (int c = 0; c < 6; ++c) { const float v = c < 4 ? init : 0.f; acc[i][c] = f32x4{v, v, v, v}; }
    }
    bf16x8 a0[4], a1[4];
    s16x4 bb[6][2];
#define RDA(A, i, u, sa) A[i] = lds_rd128(lds + (sa) + rdA[u] + (i) * 2048)
#define RDBH(c, t, sb) bb[c][t] = lds_tr(lds + (sb) + rdB[(c) & 1] + ((c) >> 1) * 512 + (t) * 1536)
#define RDB(c, sb) do { RDBH(c, 0, sb); RDBH(c, 1, sb); } while (0)
#define MM(A, i, c) do { \
        if ((c) < 2 || ((c) < 4 && !BR)) acc[i][c] = MFMA16(cat8(bb[c][0], bb[c][1]), A[i], acc[i][c]); \
        else acc[i][c] = MFMA16(A[i], cat8(bb[c][0], bb[c][1]), acc[i][c]); } while (0)
#define MR(A, i, c, RD) do { MM(A, i, c); SBAR(); RD; SBAR(); } while (0)
#define MM2(A, i, c) do { MM(A, i, c); MM(A, (i) + 1, c); } while (0)
#define LBAR() do { asm volatile("s_waitcnt lgkmcnt(0)\n\ts_barrier" ::: "memory"); SBAR(); } while (0)
#define PH_A(A, sb) do { \
        MR(A, 0, 0, RDBH(3, 0, sb)); MR(A, 1, 0, RDBH(3, 1, sb)); MR(A, 2, 0, RDBH(4, 0, sb)); MR(A, 3, 0, RDBH(4, 1, sb)); \
        MR(A, 0, 1, RDBH(5, 0, sb)); MR(A, 1, 1, RDBH(5, 1, sb)); MM2(A, 2, 1); MM2(A, 0, 2); MM2(A, 2, 2); SBAR(); } while (0)
#define PH_B(A, AN, un, san, sbn) do { \
        MM2(A, 0, 3); SBAR(); LBAR(); \
        MR(A, 2, 3, RDBH(0, 0, sbn)); MR(A, 3, 3, RDBH(0, 1, sbn)); MR(A, 0, 4, RDA(AN, 0, un, san)); MR(A, 1, 4, RDA(AN, 1, un, san)); \
        MR(A, 2, 4, RDA(AN, 2, un, san)); MR(A, 3, 4, RDA(AN, 3, un, san)); MR(A, 0, 5, RDBH(1, 0, sbn)); MR(A, 1, 5, RDBH(1, 1, sbn)); \
        MR(A, 2, 5, RDBH(2, 0, sbn)); MR(A, 3, 5, RDBH(2, 1, sbn)); } while (0)

    auto mainloop = [&](auto brc) {
        constexpr bool BR = decltype(brc)::value;
        STAMP(1);
        LBAR();
        STAMP(2);
        RDB(0, 0); RDA(a0, 0, 0, 0); RDA(a0, 1, 0, 0); RDA(a0, 2, 0, 0); RDA(a0, 3, 0, 0); RDB(1, 0); RDB(2, 0);
        __builtin_amdgcn_s_waitcnt(0xC07F);
        SBAR();
        unsigned sa = 0, sb = 0;
#pragma unroll 1
        for (int t = 0; t < 16; ++t) {
            const unsigned sb1 = NEXT_B(sb), sb2 = NEXT_B(sb1), sa1 = NEXT_A(sa);
            PH_A(a0, sb);
            PH_B(a0, a1, 1, sa, sb1);
            PH_A(a1, sb1);
            PH_B(a1, a0, 0, sa1, sb2);
            sa = sa1; sb = sb2;
        }
    int tz = threadIdx.x; asm volatile("" : "+v"(tz));
    const int tid = tz, c16 = tz & 15, g = (tz >> 4) & 3;
    STAMP(3);
    const int tile0 = mb * 8 + wm * 2;
    const unsigned po = ((g >> 1) * 64 + (g & 1) * 32) * 16;
    const __amdgpu_buffer_rsrc_t rWS = mk_rsrc(ws, 26u << 20);
    const int hq = k1_head(br, hp, 0, hh), hk = k1_head(br, hp, 1, hh);
    if constexpr (!BR) {
        const int hv = k1_head(0, hp, 2, hh);
#pragma unroll
        for (int m = 0; m < 2; ++m) {
#pragma unroll
            for (int a = 0; a < 2; ++a) {
                st16<SC1>(rWS, WS_KV + (hk * 64 + tile0 + m) * 4096 + po + ((c16 & 7) | (a << 3) | ((c16 >> 3) << 4)) * 16,
                     pack44(acc[2 * m + a][2], acc[2 * m + a][3]));
                st16<SC1>(rWS, WS_KV + (hv * 64 + tile0 + m) * 4096 + 2048 + po + (16 * a + c16) * 16, pack44(acc[2 * m][4 + a], acc[2 * m + 1][4 + a]));
                st16<SC1>(rWS, WS_Q + (hq * 64 + tile0 + m) * 2048 + po + (16 * a + c16) * 16,
                     pack44(acc[2 * m + a][0] * QSCALE, acc[2 * m + a][1] * QSCALE));
            }
        }
    } else {
        auto phi = [](f32x4& v) {
#pragma unroll
            for (int r = 0; r < 4; r += 2) {
                const f32x2 t = {v[r], v[r + 1]};
                const f32x2 u = t * LOG2E + (-1e-6f * LOG2E);
                const f32x2 e = {__builtin_amdgcn_fmed3f(__builtin_amdgcn_exp2f(u[0]), 0.f, 1.f), __builtin_amdgcn_fmed3f(__builtin_amdgcn_exp2f(u[1]), 0.f, 1.f)};
                const f32x2 mx = {__int_as_float(max(__float_as_int(t[0]), 0x358637bd)), __int_as_float(max(__float_as_int(t[1]), 0x358637bd))};
                const f32x2 o2 = mx + e;
                v[r] = o2[0]; v[r + 1] = o2[1];
            }
        };
        f32x4 kvt[2][2] = {}, zt[2] = {};
        bf16x8 ones;
#pragma unroll
        for (int e = 0; e < 8; ++e) ones[e] = (short)0x3F80;
#pragma unroll
        for (int m = 0; m < 2; ++m) {
            bf16x8 vf[2];
#pragma unroll
            for (int b = 0; b < 2; ++b) vf[b] = __builtin_bit_cast(bf16x8, pack44(acc[2 * m][4 + b], acc[2 * m + 1][4 + b]));
#pragma unroll
            for (int b = 0; b < 2; ++b) {
                phi(acc[2 * m][2 + b]); phi(acc[2 * m + 1][2 + b]);
                const bf16x8 pk = __builtin_bit_cast(bf16x8, pack44(acc[2 * m][2 + b], acc[2 * m + 1][2 + b]));
                kvt[b][0] = MFMA16(pk, vf[0], kvt[b][0]);
                kvt[b][1] = MFMA16(pk, vf[1], kvt[b][1]);
                zt[b] = MFMA16(pk, ones, zt[b]);
            }
        }
#pragma unroll
        for (int i = 0; i < 2; ++i) {
            phi(acc[i][0]); phi(acc[i][1]);
            st16<SC1>(rWS, WS_PQ + (hq * 64 + tile0) * 2048 + po + (16 * i + c16) * 16, pack44(acc[i][0], acc[i][1]));
        }
        lds_char* red = (lds_char*)smem + K1_ASLOT;
#pragma unroll
        for (int e = 0; e < 2; ++e) {
            lds_char* rp = red + wid * 4096 + (g >> 1) * 2048 + ((g & 1) * 32 + 16 * e + c16) * 8;
            *LDSP(f32x2, rp) = f32x2{kvt[0][e][0], kvt[0][e][1]};
            *LDSP(f32x2, rp + 512) = f32x2{kvt[0][e][2], kvt[0][e][3]};
            *LDSP(f32x2, rp + 1024) = f32x2{kvt[1][e][0], kvt[1][e][1]};
            *LDSP(f32x2, rp + 1536) = f32x2{kvt[1][e][2], kvt[1][e][3]};
        }
        if (c16 == 0) {
            lds_char* zp = red + 32768 + wid * 128 + (g & 1) * 64 + (g >> 1) * 32;
            *LDSP(f32x4, zp) = zt[0];
            *LDSP(f32x4, zp + 16) = zt[1];
        }
#pragma unroll
        for (int i = 2; i < 4; ++i) {
            phi(acc[i][0]); phi(acc[i][1]);
            st16<SC1>(rWS, WS_PQ + (hq * 64 + tile0 + 1) * 2048 + po + (16 * (i - 2) + c16) * 16, pack44(acc[i][0], acc[i][1]));
        }
        asm volatile("s_waitcnt lgkmcnt(0)\n\ts_barrier" ::: "memory");
        {
            const int idx = tid & 255;
            f32x4 s4 = *LDSP(const f32x4, red + (hh * 4 + 0) * 4096 + idx * 16);
#pragma unroll
            for (int m = 1; m < 4; ++m) s4 += *LDSP(const f32x4, red + (hh * 4 + m) * 4096 + idx * 16);
            st16<SC1>(rWS, WS_KVP + ((hk * 8 + mb) * 256 + idx) * 16, __builtin_bit_cast(u32x4, s4));
            if (idx < 32) {
                float z = 0.f;
#pragma unroll
                for (int m = 0; m < 4; ++m) z += *LDSP(const float, red + 32768 + (hh * 4 + m) * 128 + idx * 4);
                ((float*)(ws + WS_ZP))[(hk * 8 + mb) * 32 + idx] = z;
            }
        }
    }
    };
    if (br) mainloop(std::integral_constant<bool, true>{}); else mainloop(std::integral_constant<bool, false>{});
    STAMP_FLUSH(0);
#undef RDA
#undef RDB
#undef MM
#undef MM2
#undef MR
#undef RDBH
#undef PH_A
#undef PH_B
#undef LBAR
#undef K1_DMA_A
#undef K1_DMA_B
}

__device__ __noinline__ __attribute__((used)) void code_mark_c() { asm volatile(""); }

extern "C" void kernel_launch(void* const* d_in, const int* in_sizes, int n_in, void* d_out, int out_size, void* d_ws,
                              size_t ws_size, hipStream_t stream) {
    const unsigned short* xA = (const unsigned short*)d_in[0];
    const unsigned short* wqkvA = (const unsigned short*)d_in[1];
    const unsigned short* woA = (const unsigned short*)d_in[2];
    const unsigned short* xB = (const unsigned short*)d_in[3];
    const unsigned short* wqkvB = (const unsigned short*)d_in[4];
    const unsigned short* woB = (const unsigned short*)d_in[5];
    char* ws = (char*)d_ws;
    (void)hipFuncSetAttribute((const void*)qkv_kernel, hipFuncAttributeMaxDynamicSharedMemorySize, K1_LDS);
    (void)hipFuncSetAttribute((const void*)attn_kernel, hipFuncAttributeMaxDynamicSharedMemorySize, K23_LDS);
    qkv_kernel<<<256, 768, K1_LDS, stream>>>(xA, wqkvA, xB, wqkvB, ws);
    attn_kernel<<<256, 512, K23_LDS, stream>>>(ws, woA, woB, (unsigned short*)d_out);
}
```

```cpp
#include <hip/hip_runtime.h>
#include <hip/hip_bf16.h>

typedef __attribute__((ext_vector_type(8))) short bf16x8;
typedef __attribute__((ext_vector_type(4))) short s16x4;
typedef __attribute__((ext_vector_type(16))) float f32x16;
typedef __attribute__((ext_vector_type(4))) float f32x4;
typedef __attribute__((ext_vector_type(2))) float f32x2;
typedef __attribute__((ext_vector_type(4))) unsigned u32x4;
typedef __attribute__((address_space(3))) char lds_char;
#define LDSP(T, p) ((__attribute__((address_space(3))) T*)(p))

#define DI __device__ __forceinline__
#define MFMA32(a, b, c) __builtin_amdgcn_mfma_f32_32x32x16_bf16((a), (b), (c), 0, 0, 0)
#define MFMA16(a, b, c) __builtin_amdgcn_mfma_f32_16x16x32_bf16((a), (b), (c), 0, 0, 0)
#define SBAR() __builtin_amdgcn_sched_barrier(0)
#define PIN(x) asm volatile("" : "+v"(x))
#define WAIT_BAR(N) asm volatile("s_waitcnt vmcnt(" #N ") lgkmcnt(0)\n\ts_barrier" ::: "memory")

constexpr int SEQ = 2048, DM = 1024;
constexpr float LOG2E = 1.4426950408889634f;
constexpr float QSCALE = 0.17677669529663687f * LOG2E;

constexpr size_t WS_Q = 0;
constexpr size_t WS_KV = (size_t)4 << 20;
constexpr size_t WS_PQ = (size_t)12 << 20;
constexpr size_t WS_O = (size_t)16 << 20;
constexpr size_t WS_KVP = (size_t)24 << 20;
constexpr size_t WS_ZP = (size_t)25 << 20;
constexpr size_t WS_CNT = WS_ZP + 65536;

typedef __attribute__((ext_vector_type(2))) __bf16 bf16x2_t;
DI unsigned cvtpk(float lo, float hi) {
    return __builtin_bit_cast(unsigned, __builtin_convertvector((f32x2{lo, hi}), bf16x2_t));
}
DI bf16x8 pack8(const f32x16& x, int s) {
    u32x4 p;
    p[0] = cvtpk(x[8 * s + 0], x[8 * s + 1]);
    p[1] = cvtpk(x[8 * s + 2], x[8 * s + 3]);
    p[2] = cvtpk(x[8 * s + 4], x[8 * s + 5]);
    p[3] = cvtpk(x[8 * s + 6], x[8 * s + 7]);
    return __builtin_bit_cast(bf16x8, p);
}
DI void dma16(__amdgpu_buffer_rsrc_t rsrc, unsigned voff, unsigned soff, unsigned lds_dst) {
    unsigned keep;
    asm volatile("s_mov_b32 %0, m0\n\ts_mov_b32 m0, %4\n\ts_nop 0\n\tbuffer_load_dwordx4 %1, %2, %3 offen lds\n\ts_mov_b32 m0, %0"
                 : "=&s"(keep) : "v"(voff), "s"(rsrc), "s"(soff), "s"(lds_dst) : "memory");
}
DI void dma16_nt(__amdgpu_buffer_rsrc_t rsrc, unsigned voff, unsigned soff, unsigned lds_dst) {
    unsigned keep;
    asm volatile("s_mov_b32 %0, m0\n\ts_mov_b32 m0, %4\n\ts_nop 0\n\tbuffer_load_dwordx4 %1, %2, %3 offen nt lds\n\ts_mov_b32 m0, %0"
                 : "=&s"(keep) : "v"(voff), "s"(rsrc), "s"(soff), "s"(lds_dst) : "memory");
}
DI void dma16_sc1(__amdgpu_buffer_rsrc_t rsrc, unsigned voff, unsigned soff, unsigned lds_dst) {
    unsigned keep;
    asm volatile("s_mov_b32 %0, m0\n\ts_mov_b32 m0, %4\n\ts_nop 0\n\tbuffer_load_dwordx4 %1, %2, %3 offen sc1 lds\n\ts_mov_b32 m0, %0"
                 : "=&s"(keep) : "v"(voff), "s"(rsrc), "s"(soff), "s"(lds_dst) : "memory");
}
DI __amdgpu_buffer_rsrc_t mk_rsrc(const void* p, unsigned bytes) {
    return __builtin_amdgcn_make_buffer_rsrc((void*)p, (short)0, (int)bytes, 0x00020000);
}
constexpr int SC1 = 16;
template <int AUX> DI void st16(__amdgpu_buffer_rsrc_t rsrc, unsigned voff, u32x4 v) { __builtin_amdgcn_raw_buffer_store_b128(v, rsrc, voff, 0, AUX); }
template <int AUX> DI void st16(__amdgpu_buffer_rsrc_t rsrc, unsigned voff, bf16x8 v) { st16<AUX>(rsrc, voff, __builtin_bit_cast(u32x4, v)); }
#define ASM_LD16(dst, rsrc, voff, soff) asm volatile("buffer_load_dwordx4 %0, %1, %2, %3 offen" : "=v"(dst) : "v"(voff), "s"(rsrc), "s"(soff) : "memory")
#define ASM_LD8(dst, rsrc, voff, soff) asm volatile("buffer_load_dwordx2 %0, %1, %2, %3 offen" : "=v"(dst) : "v"(voff), "s"(rsrc), "s"(soff) : "memory")
DI bf16x8 lds_rd128(const lds_char* p) { return *LDSP(const bf16x8, p); }
DI s16x4 lds_tr(const lds_char* p) {
    typedef short v4i16_t __attribute__((ext_vector_type(4)));
    return __builtin_bit_cast(s16x4, __builtin_amdgcn_ds_read_tr16_b64_v4i16(LDSP(v4i16_t, p)));
}
DI bf16x8 cat8(s16x4 lo, s16x4 hi) { return __builtin_shufflevector(lo, hi, 0, 1, 2, 3, 4, 5, 6, 7); }
DI unsigned rfl(unsigned x) { return __builtin_amdgcn_readfirstlane(x); }

__device__ __noinline__ __attribute__((used)) void code_mark_a() { asm volatile(""); }
__device__ __noinline__ __attribute__((used)) void code_mark_b();
__device__ __noinline__ __attribute__((used)) void code_mark_c();
DI void line_touch(const void* beg, const void* end, int lane, unsigned lds_sink) {
    const long len = (const char*)end - (const char*)beg;
    const __amdgpu_buffer_rsrc_t r = mk_rsrc(beg, len > 0 && len <= 16384 * 64 ? (unsigned)len : 0u);
    const unsigned vo = lane * 128;
    unsigned keep;
    asm volatile("s_mov_b32 %0, m0\n\ts_mov_b32 m0, %4\n\ts_nop 4\n\tbuffer_load_dword %1, %2, 0 offen lds\n\tbuffer_load_dword %1, %2, %3 offen lds\n\ts_mov_b32 m0, %0"
                 : "=&s"(keep) : "v"(vo), "s"(r), "s"(8192u), "s"(lds_sink) : "memory");
}

DI u32x4 pack44(const f32x4& a, const f32x4& b) {
    u32x4 p;
    p[0] = cvtpk(a[0], a[1]); p[1] = cvtpk(a[2], a[3]); p[2] = cvtpk(b[0], b[1]); p[3] = cvtpk(b[2], b[3]);
    return p;
}

constexpr int K2_SLOT = 16384, K2_XOFF = 8 * K2_SLOT;
constexpr unsigned K2_BARS = 0x1117;

DI float xhalf_max(float m) {
    auto rr = __builtin_amdgcn_permlane32_swap(__float_as_uint(m), __float_as_uint(m), false, false);
    return __builtin_fmaxf(__uint_as_float(rr[0]), __uint_as_float(rr[1]));
}
DI float xhalf_sum(float m) {
    auto rr = __builtin_amdgcn_permlane32_swap(__float_as_uint(m), __float_as_uint(m), false, false);
    return __uint_as_float(rr[0]) + __uint_as_float(rr[1]);
}
DI float max16(const f32x16& s) {
    float a = __builtin_fmaxf(s[0], s[1]);
#pragma unroll
    for (int r = 2; r < 16; ++r) a = __builtin_fmaxf(a, s[r]);
    return a;
}

constexpr int K3_KG = 1032, K3_WSLOT = 16 * K3_KG, K3_OOFF = 4 * K3_WSLOT, K3_OSLOT = 16384, K23_LDS = K3_OOFF + 5 * K3_OSLOT;
#define NEXT_O(s) ((s) == 4 * K3_OSLOT ? 0u : (s) + K3_OSLOT)

__global__ __launch_bounds__(512, 2) void attn_kernel(char* __restrict__ ws, const unsigned short* __restrict__ woA,
                                                      const unsigned short* __restrict__ woB, unsigned short* __restrict__ out) {
    extern __shared__ __attribute__((aligned(16))) char smem[];
    const lds_char* lds = (const lds_char*)smem;
    const unsigned lds0 = (unsigned)(size_t)lds;
    const int tid = threadIdx.x, lane = tid & 63;
    const int wid = rfl(tid >> 6);
    const int hi = lane >> 5;
    const int xg = blockIdx.x & 7, j0 = blockIdx.x >> 3;
    const int head = xg * 4 + (j0 >> 3), qb = j0 & 7;
    const int br = xg >> 2, rb = (xg & 3) * 4 + (j0 >> 3), cb = j0 & 7;
    unsigned* cnt = (unsigned*)(ws + WS_CNT);
    const __amdgpu_buffer_rsrc_t rWS = mk_rsrc(ws, 26u << 20);
    const __amdgpu_buffer_rsrc_t rKV = mk_rsrc(ws + WS_KV + (size_t)head * 64 * 4096, 64 * 4096);
    const unsigned vl16 = lane * 16;
#define BF(x) __builtin_bit_cast(bf16x8, x)

    if (wid >= 4) {
        const int hw = wid - 4;
        const unsigned vo = hw * 4096 + vl16, dKV = lds0 + hw * 4096;
#define K2_DMA(st) do { const unsigned d_ = dKV + ((st) & 7) * K2_SLOT; \
            dma16(rKV, vo, (unsigned)(st) * K2_SLOT + 4096u, d_); dma16(rKV, vo, (unsigned)(st) * K2_SLOT + 4096u + 1024u, d_ + 1024); \
            dma16(rKV, vo, (unsigned)(st) * K2_SLOT + 4096u + 2048u, d_ + 2048); dma16(rKV, vo, (unsigned)(st) * K2_SLOT + 3072u, d_ + 3072); } while (0)
        K2_DMA(0); K2_DMA(1);
        WAIT_BAR(4);
        K2_DMA(2); K2_DMA(3);
        WAIT_BAR(8);
        K2_DMA(4); K2_DMA(5);
        WAIT_BAR(8);
        K2_DMA(6); K2_DMA(7);
        f32x2 kp[2][8];
        f32x4 zp[8];
        bf16x8 pq[4];
        {
            const unsigned vk = lane * 8, vz = hi * 64;
#pragma unroll
            for (int m = 0; m < 8; ++m) {
                ASM_LD8(kp[0][m], rWS, vk, (unsigned)(WS_KVP + (head * 8 + m) * 4096 + hw * 1024));
                ASM_LD8(kp[1][m], rWS, vk, (unsigned)(WS_KVP + (head * 8 + m) * 4096 + hw * 1024 + 512));
                ASM_LD16(zp[m], rWS, vz, (unsigned)(WS_ZP + (head * 8 + m) * 128 + hw * 16));
            }
#pragma unroll
            for (int i = 0; i < 4; ++i) ASM_LD16(pq[i], rWS, vl16, (unsigned)(WS_PQ + (head * 64 + qb * 8 + hw * 2) * 2048 + i * 1024));
        }
        WAIT_BAR(28);
        K2_DMA(8); K2_DMA(9); K2_DMA(10); K2_DMA(11);
        asm volatile("s_waitcnt vmcnt(16)"
                     : "+v"(kp[0][0]), "+v"(kp[0][1]), "+v"(kp[0][2]), "+v"(kp[0][3]), "+v"(kp[0][4]), "+v"(kp[0][5]), "+v"(kp[0][6]), "+v"(kp[0][7]),
                       "+v"(kp[1][0]), "+v"(kp[1][1]), "+v"(kp[1][2]), "+v"(kp[1][3]), "+v"(kp[1][4]), "+v"(kp[1][5]), "+v"(kp[1][6]), "+v"(kp[1][7]) :: "memory");
        asm volatile("" : "+v"(zp[0]), "+v"(zp[1]), "+v"(zp[2]), "+v"(zp[3]), "+v"(zp[4]), "+v"(zp[5]), "+v"(zp[6]), "+v"(zp[7]),
                          "+v"(pq[0]), "+v"(pq[1]), "+v"(pq[2]), "+v"(pq[3]));
        {
            f32x2 a0 = kp[0][0], a1 = kp[1][0];
            f32x4 z = zp[0];
#pragma unroll
            for (int m = 1; m < 8; ++m) { a0 += kp[0][m]; a1 += kp[1][m]; z += zp[m]; }
            typedef __attribute__((ext_vector_type(2))) unsigned u32x2;
            lds_char* xs = (lds_char*)smem + K2_XOFF + (hw >> 1) * 1024 + vl16 + (hw & 1) * 8;
            *LDSP(u32x2, xs) = u32x2{cvtpk(a0[0], a0[1]), cvtpk(a1[0], a1[1])};
            *LDSP(u32x2, xs + 2048) = u32x2{cvtpk(z[0], z[1]), cvtpk(z[2], z[3])};
        }
        WAIT_BAR(0);
        K2_DMA(12); K2_DMA(13); K2_DMA(14); K2_DMA(15);
        {
            const char* wb = (const char*)((xg >> 2) ? woB : woA) + j0 * 65536 + hw * 16384;
            line_touch(wb, wb + 16384, lane, lds0 + K2_XOFF + 4096);
        }
        {
            const lds_char* xs = lds + K2_XOFF + vl16;
            const bf16x8 kv0 = lds_rd128(xs), kv1 = lds_rd128(xs + 1024), z0 = lds_rd128(xs + 2048), z1 = lds_rd128(xs + 3072);
#pragma unroll
            for (int u = 0; u < 2; ++u) {
                f32x16 num = {}, den = {};
                num = MFMA32(kv0, pq[2 * u], num); den = MFMA32(z0, pq[2 * u], den);
                num = MFMA32(kv1, pq[2 * u + 1], num); den = MFMA32(z1, pq[2 * u + 1], den);
                const float rd = __builtin_amdgcn_rcpf(den[0] + 1e-6f);
                num *= rd;
                const unsigned ob = WS_O + ((64 + qb * 8 + hw * 2 + u) * 32 + head) * 2048 + vl16;
                st16<SC1>(rWS, ob, pack8(num, 0));
                st16<SC1>(rWS, ob + 1024, pack8(num, 1));
            }
        }
        asm volatile("s_waitcnt vmcnt(%0) lgkmcnt(0)\n\ts_barrier" :: "i"(2 + 4) : "memory");
        asm volatile("s_waitcnt vmcnt(0)" ::: "memory");
        if (lane == 0) __hip_atomic_fetch_add(cnt + (16 + qb * 2 + (hw >> 1)) * 32, 1u, __ATOMIC_RELAXED, __HIP_MEMORY_SCOPE_AGENT);

        const __amdgpu_buffer_rsrc_t rW3 = mk_rsrc(br ? woB : woA, DM * DM * 2);
        const __amdgpu_buffer_rsrc_t rO = mk_rsrc(ws + WS_O + (size_t)(br * 64 + rb * 4 + hw) * 65536, 65536);
        const unsigned voW = (hw * 16 + ((lane >> 2) & 3)) * (DM * 2) + (cb * 128 + (lane >> 4) * 32 + (lane & 3) * 8) * 2;
        const unsigned dW = lds0 + hw * 4 * K3_KG, dO = lds0 + K3_OOFF + hw * 4096;
#define K3_DW(i) do { _Pragma("unroll") for (int n_ = 0; n_ < 4; ++n_) \
            dma16(rW3, voW, (unsigned)(i) * (64u * DM * 2) + n_ * (4u * DM * 2), dW + ((i) & 3) * K3_WSLOT + n_ * K3_KG); } while (0)
#define K3_DO(i, so) do { _Pragma("unroll") for (int n_ = 0; n_ < 4; ++n_) \
            dma16_sc1(rO, vl16, (unsigned)(i) * 4096u + n_ * 1024u, dO + (so) + n_ * 1024); } while (0)
        K3_DW(0); K3_DW(1); K3_DW(2);
        auto wait_rows = [&]() {
            const unsigned* c = cnt + (br * 16 + rb) * 32;
            for (unsigned spin = 0; spin < (1u << 17); ++spin) {
                if (rfl(__hip_atomic_load(c, __ATOMIC_RELAXED, __HIP_MEMORY_SCOPE_AGENT)) >= 64u) break;
                __builtin_amdgcn_s_sleep(6);
            }
        };
        if (hw == 0 && br) wait_rows();
        asm volatile("s_barrier" ::: "memory");
        if (!br) {
            K3_DW(3);
            if (hw == 0) wait_rows();
        }
        asm volatile("s_barrier" ::: "memory");
        if (br) K3_DW(3);
        K3_DO(0, 0); K3_DO(1, K3_OSLOT);
        WAIT_BAR(4);
        K3_DO(2, 2 * K3_OSLOT); K3_DO(3, 3 * K3_OSLOT); K3_DO(4, 4 * K3_OSLOT);
        WAIT_BAR(12);
        K3_DW(4); K3_DO(5, 0);
        WAIT_BAR(16);
        unsigned so = K3_OSLOT;
#pragma unroll 1
        for (int i = 2; i < 12; ++i) {
            K3_DW(i + 3); K3_DO(i + 4, so);
            so = NEXT_O(so);
            WAIT_BAR(20);
        }
        K3_DW(15);
        WAIT_BAR(16);
        WAIT_BAR(8);
        WAIT_BAR(0);
        return;
    }

    const int qt0 = qb * 8 + wid * 2;
    bf16x8 qa0, qa1, qb0, qb1, kx0, kx1, ky0, ky1, v0, v1;
    {
        const unsigned sq = WS_Q + (head * 64 + qt0) * 2048;
        ASM_LD16(qa0, rWS, vl16, sq); ASM_LD16(qa1, rWS, vl16, sq + 1024);
        ASM_LD16(kx0, rKV, vl16, 0u); ASM_LD16(kx1, rKV, vl16, 1024u);
        ASM_LD16(qb0, rWS, vl16, sq + 2048); ASM_LD16(qb1, rWS, vl16, sq + 3072);
        ASM_LD16(v0, rKV, vl16, 2048u);
    }
    bf16x8 sel;
    {
        const short v = (((lane >> 4) & 1) == (((lane & 15) >> 2) & 1)) ? (short)0x3F80 : (short)0;
#pragma unroll
        for (int e = 0; e < 8; ++e) sel[e] = v;
    }
    const lds_char* fb = lds + vl16;
    f32x16 sA, sB, nmA, nmB, oA = {}, oB = {};
    f32x4 lA = {}, lB = {};
    u32x4 p0, p1 = {};
    PIN(oA); PIN(oB); PIN(lA); PIN(lB); PIN(p1); PIN(sel);
    asm volatile("s_waitcnt vmcnt(0)" : "+v"(qa0), "+v"(qa1), "+v"(qb0), "+v"(qb1), "+v"(kx0), "+v"(kx1), "+v"(v0) :: "memory");
    {
        f32x16 z16 = {};
        sA = MFMA32(kx0, qa0, z16); sB = MFMA32(kx0, qb0, z16);
        sA = MFMA32(kx1, qa1, sA); sB = MFMA32(kx1, qb1, sB);
        const float ma = xhalf_max(max16(sA)), mb = xhalf_max(max16(sB));
#pragma unroll
        for (int r = 0; r < 16; ++r) { nmA[r] = -ma; nmB[r] = -mb; sA[r] -= ma; }
    }
    PIN(nmA); PIN(nmB);
    v1 = v0;

#define EX(X, i) X[i] = __builtin_amdgcn_exp2f(X[i])
#define CV(P, n, X, i) P[n] = cvtpk(X[i], X[(i) + 1])
#define X1(op) do { op; SBAR(); } while (0)
#define ATT(SC, SN, K0, K1, Q0, Q1, NM, VP, OP, LP, VC, OC, LC, R1, R2, R3) do { \
        X1(SN = MFMA32(K0, Q0, NM)); X1(R1); \
        X1(EX(SC, 0)); X1(EX(SC, 1)); X1(p1[3] = cvtpk(e14, e15)); X1(EX(SC, 2)); \
        X1(SN = MFMA32(K1, Q1, SN)); X1(R2); \
        X1(EX(SC, 3)); X1(CV(p0, 0, SC, 0)); X1(EX(SC, 4)); X1(EX(SC, 5)); \
        X1(OP = MFMA32(VP, BF(p1), OP)); X1(R3); \
        X1(CV(p0, 1, SC, 2)); X1(EX(SC, 6)); X1(EX(SC, 7)); X1(EX(SC, 8)); \
        X1(LP = MFMA16(sel, BF(p1), LP)); \
        X1(CV(p0, 2, SC, 4)); X1(EX(SC, 9)); X1(CV(p0, 3, SC, 6)); X1(EX(SC, 10)); X1(EX(SC, 11)); \
        X1(OC = MFMA32(VC, BF(p0), OC)); \
        X1(CV(p1, 0, SC, 8)); X1(EX(SC, 12)); X1(EX(SC, 13)); X1(e14 = __builtin_amdgcn_exp2f(SC[14])); \
        X1(LC = MFMA16(sel, BF(p0), LC)); \
        X1(CV(p1, 1, SC, 10)); X1(e15 = __builtin_amdgcn_exp2f(SC[15])); X1(CV(p1, 2, SC, 12)); \
        } while (0)
#define TILE2(KC0, KC1, V0C, V1P, KN0, KN1, V0N, V1C, TP) do { \
        ATT(sA, sB, KC0, KC1, qb0, qb1, nmB, V1P, oB, lB, V0C, oA, lA, \
            (V0N = lds_rd128((TP) + 2048), V1C = lds_rd128((TP) + 3072)), KN1 = lds_rd128((TP) + 1024), KN0 = lds_rd128(TP)); \
        ATT(sB, sA, KN0, KN1, qa0, qa1, nmA, V1C, oA, lA, V0C, oB, lB, (void)0, (void)0, (void)0); } while (0)
    bf16x8 vy0, vy1;
    float e14 = 0.f, e15 = 0.f;
#pragma unroll 1
    for (int k = 0; k < 16; ++k) {
        if ((K2_BARS >> k) & 1) asm volatile("s_waitcnt lgkmcnt(0)\n\ts_barrier" ::: "memory");
        SBAR();
        const lds_char* cur = fb + (k & 7) * K2_SLOT;
        TILE2(kx0, kx1, v0, v1, ky0, ky1, vy0, vy1, cur);
        TILE2(ky0, ky1, vy0, vy1, kx0, kx1, v0, v1, cur + 4096);
        TILE2(kx0, kx1, v0, v1, ky0, ky1, vy0, vy1, cur + 2 * 4096);
        TILE2(ky0, ky1, vy0, vy1, kx0, kx1, v0, v1, cur + 3 * 4096);
    }
    p1[3] = cvtpk(e14, e15);
    oB = MFMA32(v1, BF(p1), oB);
    lB = MFMA16(sel, BF(p1), lB);
    auto finish = [&](f32x16 o, float l, const bf16x8 q0, const bf16x8 q1, int qt) {
        f32x2 t2 = {o[0], o[1]};
#pragma unroll
        for (int r = 2; r < 16; r += 2) t2 += f32x2{o[r], o[r + 1]};
        const bool bad = !(l < 1.2676506e30f) || !(__builtin_fabsf(t2[0] + t2[1]) < 3.0e38f);
        if (__builtin_expect(__any(bad), 0)) {
            const u32x4* KV = (const u32x4*)(ws + WS_KV) + (size_t)head * 64 * 256 + lane;
            float m = -INFINITY, ls = 0.f;
#pragma unroll
            for (int r = 0; r < 16; ++r) o[r] = 0.f;
#pragma unroll 1
            for (int jt = 0; jt < 64; ++jt) {
                const bf16x8 a0 = BF(KV[jt * 256]), a1 = BF(KV[jt * 256 + 64]), b0 = BF(KV[jt * 256 + 128]), b1 = BF(KV[jt * 256 + 192]);
                f32x16 sc = {};
                sc = MFMA32(a0, q0, sc); sc = MFMA32(a1, q1, sc);
                const float mn = __builtin_fmaxf(m, xhalf_max(max16(sc)));
                const float al = __builtin_amdgcn_exp2f(m - mn);
                m = mn; ls *= al;
#pragma unroll
                for (int r = 0; r < 16; ++r) { o[r] *= al; sc[r] = __builtin_amdgcn_exp2f(sc[r] - mn); ls += sc[r]; }
                o = MFMA32(b0, pack8(sc, 0), o); o = MFMA32(b1, pack8(sc, 1), o);
            }
            l = xhalf_sum(ls);
        }
        o *= __builtin_amdgcn_rcpf(l);
        const unsigned ob = WS_O + (qt * 32 + head) * 2048 + vl16;
        st16<SC1>(rWS, ob, pack8(o, 0));
        st16<SC1>(rWS, ob + 1024, pack8(o, 1));
    };
    finish(oA, lA[0], qa0, qa1, qt0);
    finish(oB, lB[0], qb0, qb1, qt0 + 1);
    asm volatile("s_waitcnt vmcnt(0)" ::: "memory");
    if (lane == 0) __hip_atomic_fetch_add(cnt + (qb * 2 + (wid >> 1)) * 32, 1u, __ATOMIC_RELAXED, __HIP_MEMORY_SCOPE_AGENT);
#undef EX
#undef ATT
#undef CV
#undef TILE2
#undef X1
#undef K2_DMA

    {
    const int wr = wid & 1, wc = wid >> 1, c16 = lane & 15, g = lane >> 4;
    const unsigned rdO = K3_OOFF + wr * 8192 + (g >> 1) * 1024 + ((g & 1) * 32 + c16) * 16;
    const unsigned rdW = ((g >> 1) * 4 + (g & 1)) * K3_KG + wc * 512 + (c16 >> 2) * 64 + (lane & 3) * 16;
    f32x4 acc[4][4] = {};
    bf16x8 o0[4], o1[4];
    s16x4 w0[4][2], w1[4][2];
#define K3_RO(O, i, pO, hd) O[i] = lds_rd128((pO) + ((i) >> 1) * 4096 + ((i) & 1) * 256 + (hd) * 2048)
#define K3_RWH(Wf, ct, t, pW, hd) Wf[ct][t] = lds_tr((pW) + (hd) * 8 * K3_KG + (t) * 2 * K3_KG + ((ct) >> 1) * 256 + ((ct) & 1) * 8)
#define K3_RW(Wf, ct, pW, hd) do { K3_RWH(Wf, ct, 0, pW, hd); K3_RWH(Wf, ct, 1, pW, hd); } while (0)
#define K3_MM(OC, WC, n) acc[(n) & 3][(n) >> 2] = MFMA16(cat8(WC[(n) >> 2][0], WC[(n) >> 2][1]), OC[(n) & 3], acc[(n) & 3][(n) >> 2])
#define K3_MR(OC, WC, n, RD) do { K3_MM(OC, WC, n); SBAR(); RD; SBAR(); } while (0)
#define K3_RUN12(OC, WC, n0, ON, WN, pO, pW, hd) do { \
        K3_MR(OC, WC, (n0) + 0, K3_RO(ON, 3, pO, hd));     K3_MR(OC, WC, (n0) + 1, K3_RO(ON, 2, pO, hd)); \
        K3_MR(OC, WC, (n0) + 2, K3_RO(ON, 1, pO, hd));     K3_MR(OC, WC, (n0) + 3, K3_RO(ON, 0, pO, hd)); \
        K3_MR(OC, WC, (n0) + 4, K3_RWH(WN, 0, 0, pW, hd)); K3_MR(OC, WC, (n0) + 5, K3_RWH(WN, 0, 1, pW, hd)); \
        K3_MR(OC, WC, (n0) + 6, K3_RWH(WN, 1, 0, pW, hd)); K3_MR(OC, WC, (n0) + 7, K3_RWH(WN, 1, 1, pW, hd)); \
        K3_MR(OC, WC, (n0) + 8, K3_RWH(WN, 2, 0, pW, hd)); K3_MR(OC, WC, (n0) + 9, K3_RWH(WN, 2, 1, pW, hd)); \
        K3_MR(OC, WC, (n0) + 10, K3_RWH(WN, 3, 0, pW, hd)); K3_MR(OC, WC, (n0) + 11, K3_RWH(WN, 3, 1, pW, hd)); } while (0)
#define LBAR() do { asm volatile("s_waitcnt lgkmcnt(0)\n\ts_barrier" ::: "memory"); SBAR(); } while (0)
    LBAR();
    LBAR();
    LBAR();
    const lds_char* pO = lds + rdO;
    const lds_char* pW = lds + rdW;
    K3_RW(w0, 0, pW, 0); K3_RO(o0, 0, pO, 0); K3_RO(o0, 1, pO, 0); K3_RO(o0, 2, pO, 0); K3_RO(o0, 3, pO, 0); K3_RW(w0, 1, pW, 0); K3_RW(w0, 2, pW, 0); K3_RW(w0, 3, pW, 0);
    __builtin_amdgcn_s_waitcnt(0xC07F);
    SBAR();
    unsigned so = 0, sw = 0;
#pragma unroll 1
    for (int i = 0; i < 15; ++i) {
        K3_RUN12(o0, w0, 0, o1, w1, pO, pW, 1);
        K3_MR(o0, w0, 12, so = NEXT_O(so));
        K3_MR(o0, w0, 13, sw = (sw == 3 * K3_WSLOT ? 0u : sw + K3_WSLOT));
        K3_MR(o0, w0, 14, pO = lds + rdO + so; PIN(pO));
        K3_MR(o0, w0, 15, pW = lds + rdW + sw; PIN(pW));
        K3_MM(o1, w1, 0); K3_MM(o1, w1, 1); K3_MM(o1, w1, 2); K3_MM(o1, w1, 3); SBAR();
        LBAR();
        K3_RUN12(o1, w1, 4, o0, w0, pO, pW, 0);
    }
    K3_RUN12(o0, w0, 0, o1, w1, pO, pW, 1);
    K3_MM(o0, w0, 12); K3_MM(o0, w0, 13); K3_MM(o0, w0, 14); K3_MM(o0, w0, 15);
#pragma unroll
    for (int n = 0; n < 16; ++n) K3_MM(o1, w1, (n >> 2) | ((n & 3) << 2));
    {
        const __amdgpu_buffer_rsrc_t rOut = mk_rsrc(out, SEQ * 2 * DM * 2);
        const unsigned ob = ((rb * 128 + wr * 64 + c16) * (2 * DM) + br * DM + cb * 128 + wc * 64 + g * 8) * 2;
#pragma unroll
        for (int i = 0; i < 4; ++i)
#pragma unroll
            for (int u = 0; u < 2; ++u)
                st16<SC1>(rOut, ob + i * 16 * (2 * DM * 2) + u * 64, pack44(acc[i][2 * u], acc[i][2 * u + 1]));
    }
    }
#undef K3_DW
#undef K3_DO
#undef K3_RO
#undef K3_RW
#undef K3_RWH
#undef K3_MR
#undef K3_MM
#undef K3_RUN12
#undef LBAR
}

__device__ __noinline__ __attribute__((used)) void code_mark_b() { asm volatile(""); }

constexpr int K1_ASLOT = 32768, K1_BSLOT = 12288, K1_BOFF = 3 * K1_ASLOT, K1_LDS = K1_BOFF + 5 * K1_BSLOT + 256;

DI int k1_head(int br, int hp, int sec, int hh) {
    const int off = sec == 0 ? 0 : (sec == 1 || br) ? 1 : 2;
    return ((hp & 12) + ((hp + off) & 3)) * 2 + hh;
}
__global__ __launch_bounds__(768) void qkv_kernel(const unsigned short* __restrict__ xA, const unsigned short* __restrict__ wA,
                                                  const unsigned short* __restrict__ xB, const unsigned short* __restrict__ wB,
                                                  char* __restrict__ ws) {
    extern __shared__ __attribute__((aligned(16))) char smem[];
    const lds_char* lds = (const lds_char*)smem;
    const unsigned lds0 = (unsigned)(size_t)lds;
    const int tid = threadIdx.x, lane = tid & 63;
    const int wid = rfl(tid >> 6), wm = wid & 3, hh = (wid >> 2) & 1;
    const int xg = blockIdx.x & 7, j = blockIdx.x >> 3;
    const int br = (j >> 2) & 1;
    const int mb = (xg & 1) * 4 + (j & 3);
    const int hp = (xg >> 1) * 4 + (j >> 3);
#define NEXT_A(s) ((s) == 2 * K1_ASLOT ? 0u : (s) + K1_ASLOT)
#define NEXT_B(s) ((s) == 4 * K1_BSLOT ? 0u : (s) + K1_BSLOT)

    if (wid >= 8) {
        const unsigned short* X = br ? xB : xA;
        const unsigned short* W = br ? wB : wA;
        const __amdgpu_buffer_rsrc_t rX = mk_rsrc(X + (size_t)mb * 256 * DM, 256 * DM * 2);
        const __amdgpu_buffer_rsrc_t rW = mk_rsrc(W, DM * 3 * DM * 2);
        const int hw = wid - 8;
        unsigned voA[2], voB[3];
#pragma unroll
        for (int i = 0; i < 2; ++i) {
            const int R = hw * 64 + i * 8 + (lane >> 3);
            voA[i] = R * (DM * 2) + ((lane & 7) ^ ((R >> 1) & 7)) * 16;
        }
#pragma unroll
        for (int i = 0; i < 3; ++i) {
            const int blk = i * 4 + (lane >> 4), kg = hw * 2 + blk / 6, cb = blk % 6;
            const int hd = k1_head(br, hp, cb >> 1, cb & 1);
            voB[i] = (kg * 4 + ((lane >> 2) & 3)) * (3 * DM * 2) + ((cb >> 1) * DM + hd * 32 + ((lane & 3) ^ ((hw & 1) * 2)) * 8) * 2;
        }
        const unsigned dA = lds0 + hw * 8192, dB = lds0 + K1_BOFF + hw * 3072;
#define K1_DMA_A(t, slot) do { _Pragma("unroll") for (int i_ = 0; i_ < 8; ++i_) \
            dma16(rX, voA[i_ & 1], (unsigned)(t) * 128u + (i_ >> 1) * (16u * DM * 2), dA + (slot) + i_ * 1024); } while (0)
#define K1_DMA_B(u, slot) do { _Pragma("unroll") for (int i_ = 0; i_ < 3; ++i_) \
            dma16_nt(rW, voB[i_], (unsigned)(u) * (32u * 3 * DM * 2), dB + (slot) + i_ * 1024); } while (0)
        K1_DMA_B(0, 0); K1_DMA_A(0, 0);
        K1_DMA_B(1, K1_BSLOT);
        WAIT_BAR(3);
        K1_DMA_B(2, 2 * K1_BSLOT); K1_DMA_A(1, K1_ASLOT);
        K1_DMA_B(3, 3 * K1_BSLOT);
        WAIT_BAR(14);
        K1_DMA_B(4, 4 * K1_BSLOT); K1_DMA_A(2, 2 * K1_ASLOT);
        K1_DMA_B(5, 0);
        WAIT_BAR(17);
        unsigned sa = 0, sb = K1_BSLOT;
#pragma unroll 1
        for (int t = 0; t < 13; ++t) {
            K1_DMA_B(2 * t + 6, sb); K1_DMA_A(t + 3, sa);
            sb = NEXT_B(sb); sa = NEXT_A(sa);
            WAIT_BAR(25);
            K1_DMA_B(2 * t + 7, sb);
            sb = NEXT_B(sb);
            WAIT_BAR(17);
        }
        line_touch((const void*)&code_mark_a, hw == 0 ? (const void*)&code_mark_b : (const void*)&code_mark_a, lane, lds0 + K1_LDS - 256);
        WAIT_BAR(16);
        WAIT_BAR(5);
        WAIT_BAR(2);
        asm volatile("s_barrier" ::: "memory");
        return;
    }

    if (blockIdx.x == 0 && tid < 32) ((unsigned*)(ws + WS_CNT))[tid * 32] = 0u;
    if (wid == 7) line_touch((const void*)&code_mark_b, (const void*)&code_mark_c, lane, lds0 + K1_LDS - 256);
    const int c16 = lane & 15, g = lane >> 4;
    unsigned rdA[2], rdB[2];
#pragma unroll
    for (int u = 0; u < 2; ++u) {
        rdA[u] = (wm * 64 + c16) * 128 + (((4 * u + g) ^ ((c16 >> 1) & 7)) * 16);
        rdB[u] = K1_BOFF + g * 3072 + (c16 >> 2) * 64 + ((u ^ (g & 1)) * 32) + (lane & 3) * 8 + hh * 256;
    }
    f32x4 acc[4][6];
    {
        const float init = br ? 1e-6f : 0.f;
#pragma unroll
        for (int i = 0; i < 4; ++i)
#pragma unroll
            for (int c = 0; c < 6; ++c) { const float v = c < 4 ? init : 0.f; acc[i][c] = f32x4{v, v, v, v}; }
    }
    bf16x8 a0[4], a1[4];
    s16x4 bb[6][2];
#define RDA(A, i, u, sa) A[i] = lds_rd128(lds + (sa) + rdA[u] + (i) * 2048)
#define RDBH(c, t, sb) bb[c][t] = lds_tr(lds + (sb) + rdB[(c) & 1] + ((c) >> 1) * 512 + (t) * 1536)
#define RDB(c, sb) do { RDBH(c, 0, sb); RDBH(c, 1, sb); } while (0)
#define MM(A, i, c) do { \
        if ((c) < 2 || ((c) < 4 && !BR)) acc[i][c] = MFMA16(cat8(bb[c][0], bb[c][1]), A[i], acc[i][c]); \
        else acc[i][c] = MFMA16(A[i], cat8(bb[c][0], bb[c][1]), acc[i][c]); } while (0)
#define MR(A, i, c, RD) do { MM(A, i, c); SBAR(); RD; SBAR(); } while (0)
#define MM2(A, i, c) do { MM(A, i, c); MM(A, (i) + 1, c); } while (0)
#define LBAR() do { asm volatile("s_waitcnt lgkmcnt(0)\n\ts_barrier" ::: "memory"); SBAR(); } while (0)
#define PH_A(A, sb) do { \
        MR(A, 0, 0, RDBH(3, 0, sb)); MR(A, 1, 0, RDBH(3, 1, sb)); MR(A, 2, 0, RDBH(4, 0, sb)); MR(A, 3, 0, RDBH(4, 1, sb)); \
        MR(A, 0, 1, RDBH(5, 0, sb)); MR(A, 1, 1, RDBH(5, 1, sb)); MM2(A, 2, 1); MM2(A, 0, 2); MM2(A, 2, 2); SBAR(); } while (0)
#define PH_B(A, AN, un, san, sbn) do { \
        MM2(A, 0, 3); SBAR(); LBAR(); \
        MR(A, 2, 3, RDBH(0, 0, sbn)); MR(A, 3, 3, RDBH(0, 1, sbn)); MR(A, 0, 4, RDA(AN, 0, un, san)); MR(A, 1, 4, RDA(AN, 1, un, san)); \
        MR(A, 2, 4, RDA(AN, 2, un, san)); MR(A, 3, 4, RDA(AN, 3, un, san)); MR(A, 0, 5, RDBH(1, 0, sbn)); MR(A, 1, 5, RDBH(1, 1, sbn)); \
        MR(A, 2, 5, RDBH(2, 0, sbn)); MR(A, 3, 5, RDBH(2, 1, sbn)); } while (0)

    auto mainloop = [&](auto brc) {
        constexpr bool BR = decltype(brc)::value;
        LBAR();
        RDB(0, 0); RDA(a0, 0, 0, 0); RDA(a0, 1, 0, 0); RDA(a0, 2, 0, 0); RDA(a0, 3, 0, 0); RDB(1, 0); RDB(2, 0);
        __builtin_amdgcn_s_waitcnt(0xC07F);
        SBAR();
        unsigned sa = 0, sb = 0;
#pragma unroll 1
        for (int t = 0; t < 16; ++t) {
            const unsigned sb1 = NEXT_B(sb), sb2 = NEXT_B(sb1), sa1 = NEXT_A(sa);
            PH_A(a0, sb);
            PH_B(a0, a1, 1, sa, sb1);
            PH_A(a1, sb1);
            PH_B(a1, a0, 0, sa1, sb2);
            sa = sa1; sb = sb2;
        }
    int tz = threadIdx.x; asm volatile("" : "+v"(tz));
    const int tid = tz, c16 = tz & 15, g = (tz >> 4) & 3;
    const int tile0 = mb * 8 + wm * 2;
    const unsigned po = ((g >> 1) * 64 + (g & 1) * 32) * 16;
    const __amdgpu_buffer_rsrc_t rWS = mk_rsrc(ws, 26u << 20);
    const int hq = k1_head(br, hp, 0, hh), hk = k1_head(br, hp, 1, hh);
    if constexpr (!BR) {
        const int hv = k1_head(0, hp, 2, hh);
#pragma unroll
        for (int m = 0; m < 2; ++m) {
#pragma unroll
            for (int a = 0; a < 2; ++a) {
                st16<SC1>(rWS, WS_KV + (hk * 64 + tile0 + m) * 4096 + po + ((c16 & 7) | (a << 3) | ((c16 >> 3) << 4)) * 16,
                     pack44(acc[2 * m + a][2], acc[2 * m + a][3]));
                st16<SC1>(rWS, WS_KV + (hv * 64 + tile0 + m) * 4096 + 2048 + po + (16 * a + c16) * 16, pack44(acc[2 * m][4 + a], acc[2 * m + 1][4 + a]));
                st16<SC1>(rWS, WS_Q + (hq * 64 + tile0 + m) * 2048 + po + (16 * a + c16) * 16,
                     pack44(acc[2 * m + a][0] * QSCALE, acc[2 * m + a][1] * QSCALE));
            }
        }
    } else {
        auto phi = [](f32x4& v) {
#pragma unroll
            for (int r = 0; r < 4; r += 2) {
                const f32x2 t = {v[r], v[r + 1]};
                const f32x2 u = t * LOG2E + (-1e-6f * LOG2E);
                const f32x2 e = {__builtin_amdgcn_fmed3f(__builtin_amdgcn_exp2f(u[0]), 0.f, 1.f), __builtin_amdgcn_fmed3f(__builtin_amdgcn_exp2f(u[1]), 0.f, 1.f)};
                const f32x2 mx = {__int_as_float(max(__float_as_int(t[0]), 0x358637bd)), __int_as_float(max(__float_as_int(t[1]), 0x358637bd))};
                const f32x2 o2 = mx + e;
                v[r] = o2[0]; v[r + 1] = o2[1];
            }
        };
        f32x4 kvt[2][2] = {}, zt[2] = {};
        bf16x8 ones;
#pragma unroll
        for (int e = 0; e < 8; ++e) ones[e] = (short)0x3F80;
#pragma unroll
        for (int m = 0; m < 2; ++m) {
            bf16x8 vf[2];
#pragma unroll
            for (int b = 0; b < 2; ++b) vf[b] = __builtin_bit_cast(bf16x8, pack44(acc[2 * m][4 + b], acc[2 * m + 1][4 + b]));
#pragma unroll
            for (int b = 0; b < 2; ++b) {
                phi(acc[2 * m][2 + b]); phi(acc[2 * m + 1][2 + b]);
                const bf16x8 pk = __builtin_bit_cast(bf16x8, pack44(acc[2 * m][2 + b], acc[2 * m + 1][2 + b]));
                kvt[b][0] = MFMA16(pk, vf[0], kvt[b][0]);
                kvt[b][1] = MFMA16(pk, vf[1], kvt[b][1]);
                zt[b] = MFMA16(pk, ones, zt[b]);
            }
        }
#pragma unroll
        for (int i = 0; i < 2; ++i) {
            phi(acc[i][0]); phi(acc[i][1]);
            st16<SC1>(rWS, WS_PQ + (hq * 64 + tile0) * 2048 + po + (16 * i + c16) * 16, pack44(acc[i][0], acc[i][1]));
        }
        lds_char* red = (lds_char*)smem + K1_ASLOT;
#pragma unroll
        for (int e = 0; e < 2; ++e) {
            lds_char* rp = red + wid * 4096 + (g >> 1) * 2048 + ((g & 1) * 32 + 16 * e + c16) * 8;
            *LDSP(f32x2, rp) = f32x2{kvt[0][e][0], kvt[0][e][1]};
            *LDSP(f32x2, rp + 512) = f32x2{kvt[0][e][2], kvt[0][e][3]};
            *LDSP(f32x2, rp + 1024) = f32x2{kvt[1][e][0], kvt[1][e][1]};
            *LDSP(f32x2, rp + 1536) = f32x2{kvt[1][e][2], kvt[1][e][3]};
        }
        if (c16 == 0) {
            lds_char* zp = red + 32768 + wid * 128 + (g & 1) * 64 + (g >> 1) * 32;
            *LDSP(f32x4, zp) = zt[0];
            *LDSP(f32x4, zp + 16) = zt[1];
        }
#pragma unroll
        for (int i = 2; i < 4; ++i) {
            phi(acc[i][0]); phi(acc[i][1]);
            st16<SC1>(rWS, WS_PQ + (hq * 64 + tile0 + 1) * 2048 + po + (16 * (i - 2) + c16) * 16, pack44(acc[i][0], acc[i][1]));
        }
        asm volatile("s_waitcnt lgkmcnt(0)\n\ts_barrier" ::: "memory");
        {
            const int idx = tid & 255;
            f32x4 s4 = *LDSP(const f32x4, red + (hh * 4 + 0) * 4096 + idx * 16);
#pragma unroll
            for (int m = 1; m < 4; ++m) s4 += *LDSP(const f32x4, red + (hh * 4 + m) * 4096 + idx * 16);
            st16<SC1>(rWS, WS_KVP + ((hk * 8 + mb) * 256 + idx) * 16, __builtin_bit_cast(u32x4, s4));
            if (idx < 32) {
                float z = 0.f;
#pragma unroll
                for (int m = 0; m < 4; ++m) z += *LDSP(const float, red + 32768 + (hh * 4 + m) * 128 + idx * 4);
                ((float*)(ws + WS_ZP))[(hk * 8 + mb) * 32 + idx] = z;
            }
        }
    }
    };
    if (br) mainloop(std::integral_constant<bool, true>{}); else mainloop(std::integral_constant<bool, false>{});
#undef RDA
#undef RDB
#undef MM
#undef MM2
#undef MR
#undef RDBH
#undef PH_A
#undef PH_B
#undef LBAR
#undef K1_DMA_A
#undef K1_DMA_B
}

__device__ __noinline__ __attribute__((used)) void code_mark_c() { asm volatile(""); }

extern "C" void kernel_launch(void* const* d_in, const int* in_sizes, int n_in, void* d_out, int out_size, void* d_ws,
                              size_t ws_size, hipStream_t stream) {
    const unsigned short* xA = (const unsigned short*)d_in[0];
    const unsigned short* wqkvA = (const unsigned short*)d_in[1];
    const unsigned short* woA = (const unsigned short*)d_in[2];
    const unsigned short* xB = (const unsigned short*)d_in[3];
    const unsigned short* wqkvB = (const unsigned short*)d_in[4];
    const unsigned short* woB = (const unsigned short*)d_in[5];
    char* ws = (char*)d_ws;
    (void)hipFuncSetAttribute((const void*)qkv_kernel, hipFuncAttributeMaxDynamicSharedMemorySize, K1_LDS);
    (void)hipFuncSetAttribute((const void*)attn_kernel, hipFuncAttributeMaxDynamicSharedMemorySize, K23_LDS);
    qkv_kernel<<<256, 768, K1_LDS, stream>>>(xA, wqkvA, xB, wqkvB, ws);
    attn_kernel<<<256, 512, K23_LDS, stream>>>(ws, woA, woB, (unsigned short*)d_out);
}
```
